```python
import torch

def _softmax(x, axis=-1):
    return torch.softmax(x, dim=axis)
def _gelu(x):
    return torch.nn.functional.gelu(x, approximate="tanh")
def _silu(x):
    return torch.nn.functional.silu(x)
def _sigmoid(x):
    return torch.sigmoid(x)
def _layernorm(x, axis=-1, eps=1e-5):
    m = x.mean(dim=axis, keepdim=True)
    v = x.var(dim=axis, keepdim=True, unbiased=False)
    return (x - m) / torch.sqrt(v + eps)
def _scatter_add(tgt, idx, src, axis=0):
    return tgt.clone().index_add_(axis, idx, src)
def _segment_sum(src, seg, n):
    out = torch.zeros((n,) + src.shape[1:], dtype=src.dtype)
    return out.index_add_(0, seg, src)
def _pad(x, pw):
    flat = tuple(v for pair in reversed(pw) for v in pair)
    return torch.nn.functional.pad(x, flat)

kernel_name = 'gated_mix__ssm_conv_s4d__moe_arch_mod__prompt'

def setup_inputs():

    _w = (lambda a: (lambda t: t.to(torch.bfloat16) if t.is_floating_point() else t)(torch.as_tensor(a)))
    def _A_setup():
        Bsz, L, D, N = 1, 2048, 1024, 128
        return {
            "u": _w((lambda *s: torch.randn(*s, dtype=torch.bfloat16) * 0.05)(Bsz, L, D)),
            "log_A_real": _w((lambda *s: torch.randn(*s, dtype=torch.bfloat16) * 6.4)(D, N) * 0.5),
            "A_imag": _w((lambda *s: torch.randn(*s, dtype=torch.bfloat16) * 0.1)(D, N) * 3.14),
            "C_real": _w((lambda *s: torch.randn(*s, dtype=torch.bfloat16) * 0.4)(D, N) / N ** 0.5),
            "C_imag": _w((lambda *s: torch.randn(*s, dtype=torch.bfloat16) * 0.4)(D, N) / N ** 0.5),
            "log_dt": _w((lambda *s: torch.randn(*s, dtype=torch.bfloat16) * 0.1)(D) * 0.5 - 4.0),
            "D_skip": _w((lambda *s: torch.randn(*s, dtype=torch.bfloat16) * 0.1)(D)),
        }
    _A_in = _A_setup()

    def _B_setup():
        B, S, D, F, C = 1, 2048, 1024, 3584, 8
        return {
            "x": _w((lambda *s: torch.randn(*s, dtype=torch.bfloat16) * 0.05)(B, S, D)),
            "w_r": _w((lambda *s: torch.randn(*s, dtype=torch.bfloat16) * 6.4)(D)),
            "w_gate": _w((lambda *s: torch.randn(*s, dtype=torch.bfloat16) * 0.1)(D, F)),
            "w_up": _w((lambda *s: torch.randn(*s, dtype=torch.bfloat16) * 0.1)(D, F)),
            "w_down": _w((lambda *s: torch.randn(*s, dtype=torch.bfloat16) * 0.1)(F, D)),
        }
    _B_in = _B_setup()

    return {
        "A_u": _A_in["u"],
        "A_log_A_real": _A_in["log_A_real"],
        "A_A_imag": _A_in["A_imag"],
        "A_C_real": _A_in["C_real"],
        "A_C_imag": _A_in["C_imag"],
        "A_log_dt": _A_in["log_dt"],
        "A_D_skip": _A_in["D_skip"],
        "B_x": _B_in["x"],
        "B_w_r": _B_in["w_r"],
        "B_w_gate": _B_in["w_gate"],
        "B_w_up": _B_in["w_up"],
        "B_w_down": _B_in["w_down"],
        "gate_w": _w((lambda *s: torch.randn(*s, dtype=torch.bfloat16) * 0.05)(1024) * 16.0),
    }

def reference(A_u, A_log_A_real, A_A_imag, A_C_real, A_C_imag, A_log_dt, A_D_skip, B_x, B_w_r, B_w_gate, B_w_up, B_w_down, gate_w):
    def _A_ref(u, log_A_real, A_imag, C_real, C_imag, log_dt, D_skip):
        L = u.shape[1]
        dt = torch.exp(log_dt)
        A = -torch.exp(log_A_real) + 1j * A_imag
        C = (C_real + 1j * C_imag) * dt[:, None]
        pos = (lambda x, dt: x.to(dt))(torch.arange(L), log_dt.dtype)
        P = torch.exp((A * dt[:, None])[:, :, None] * pos[None, None, :])
        K = 2.0 * torch.einsum("dn,dnl->dl", C, P).real
        L2 = 2 * L
        u_t = (lambda x, *d: x.permute(*d))(u, 0, 2, 1)
        Kf = (lambda x, *a, **k: torch.fft.rfft(x.float(), *a, **k))(K, L2)
        Uf = (lambda x, *a, **k: torch.fft.rfft(x.float(), *a, **k))(u_t, L2)
        y = (lambda x, *a, **k: torch.fft.irfft(x, *a, **k).to(torch.bfloat16))(Uf * Kf[None, :, :], L2)[:, :, :L]
        y = (lambda x, *d: x.permute(*d))(y, 0, 2, 1)
        return y + u * D_skip[None, None, :]

    def _B_ref(x, w_r, w_gate, w_up, w_down):
        B, S, D = x.shape
        C = 8
        r = x @ w_r
        idx = torch.argsort(-r, axis=-1)[:, :C]
        idx_e = (lambda x, n, axis: torch.repeat_interleave(x, n, dim=axis))(idx.reshape(B, C, 1), D, axis=2)
        x_sel = (lambda x, i, axis: torch.take_along_dim(x, i, dim=axis))(x, idx_e, axis=1)
        g = _silu(x_sel @ w_gate)
        u = x_sel @ w_up
        y = (g * u) @ w_down
        r_sel = _sigmoid((lambda x, i, axis: torch.take_along_dim(x, i, dim=axis))(r, idx, axis=1))
        y = y * r_sel.reshape(B, C, 1)
        b_off = (torch.arange(B) * S).reshape(B, 1)
        flat_idx = (idx + b_off).reshape(B * C)
        zero = (x * 0.0).reshape(B * S, D)
        upd = _scatter_add(zero, flat_idx, y.reshape(B * C, D), axis=0)
        return x + upd.reshape(B, S, D)

    x = A_u
    g = _sigmoid((x * gate_w).sum(-1, keepdims=True))
    a = _A_ref(x, A_log_A_real, A_A_imag, A_C_real, A_C_imag, A_log_dt, A_D_skip)
    b = _B_ref(x, B_w_r, B_w_gate, B_w_up, B_w_down)
    return g * a + (1 - g) * b
```

```cpp
#include <hip/hip_runtime.h>
#include <stdint.h>

typedef unsigned short u16;
typedef unsigned u32;
typedef unsigned long long u64;
typedef u32 v4u __attribute__((ext_vector_type(4)));
typedef u32 v2u __attribute__((ext_vector_type(2)));
typedef float f32x2 __attribute__((ext_vector_type(2)));
typedef float f32x4 __attribute__((ext_vector_type(4)));
typedef float f32x16 __attribute__((ext_vector_type(16)));
typedef short bf16x8 __attribute__((ext_vector_type(8)));
typedef short s16x4 __attribute__((ext_vector_type(4)));
typedef __bf16 bf2_t __attribute__((ext_vector_type(2)));
#define LAS __attribute__((address_space(3)))
typedef LAS char* lptr;

#define L_ 2048
#define D_ 1024
#define F_ 3584

#define OFF_E 0
#define OFF_FL 0x1000
#define OFF_R 0x2000
#define OFF_G 0x6000
#define OFF_H2 0xA000
#define OFF_H3 0x192000
#define WS_USED 0x280000
#define OFF_STAMP 0x1000000
#define SC1 16
#define SPIN_CAP 20000
#define LSPIN_CAP 400000

#define WR 16448
#define ROWB 528
#define T_PN 5824
#define T_P2 (T_PN + 2304)
#define T_P3 (T_PN + 3584)
#define S_OFF 11648
#define U_OFF (S_OFF + 512)
#define G_OFF (8 * WR)
#define FIX_OFF (G_OFF + 8192)
#define PRM_OFF (FIX_OFF + 320)
#define MSK_OFF (PRM_OFF + 8 * 1280)
#define DUMP_OFF (MSK_OFF + 5120)
#define FL_OFF (DUMP_OFF + 1024)
#define LDS_BYTES (FL_OFF + 128)
#define F_PROG 0
#define F_FIX 16
#define F_UG 8
#define GU_OFF 0
#define XS_OFF 131072
#define PD_OFF XS_OFF
#define HS_OFF (XS_OFF + 4096)
#define KEY_OFF (HS_OFF + 8 * 272)
#define F_TAG 0
#define F_XS 1
#define F_GU 8
#define F_PP 12
#define F_H 16
#define F_PD 4

#define STAMP(k) do { } while (0)
#define STAMPC(k) do { } while (0)
#define STAMP_OUT() do { } while (0)

#define WLD(T, p) __builtin_nontemporal_load((const T*)(p))
#define WIN 7
#define PSLEEP 1
#define NGU 32
#define CODE_LINES 185
#define RDELAY 0
#define WDELAY 4
#define H2DELAY 2
#define HELP 0
#define CSLEEP 4
#define WS_BASE 0
#define BPRIO 1
#define XPH 3
#define RCAN 1
#define PERM 0
#define SPULL 0
#define DTIME 600
#define PULL_AT -1
#define H2ACK 1
#define DWIN 32
#define SAMEX 0
#define HTIME 900
#define EARLYPULL 1
#define DEFER 1
#define EARLYH2 0
#define DGATE 1
#define DAT 30
#define XLD(T, p) __builtin_nontemporal_load((const T*)(p))
#define BARRIER() asm volatile("s_waitcnt lgkmcnt(0)\n\ts_barrier" ::: "memory")
#define CFENCE() asm volatile("" ::: "memory")
#define SCHED() __builtin_amdgcn_sched_barrier(0)

__device__ __forceinline__ float bflo(u32 w) { return __uint_as_float(w << 16); }
__device__ __forceinline__ float bfhi(u32 w) { return __uint_as_float(w & 0xffff0000u); }
__device__ __forceinline__ u32 pk(float lo, float hi) {
    f32x2 v = {lo, hi};
    return __builtin_bit_cast(u32, __builtin_convertvector(v, bf2_t));
}
__device__ __forceinline__ float rbf(float f) { return bflo(pk(f, f)); }
__device__ __forceinline__ f32x4 mfma16(v4u a, v4u b, f32x4 c) {
    return __builtin_amdgcn_mfma_f32_16x16x32_bf16(__builtin_bit_cast(bf16x8, a), __builtin_bit_cast(bf16x8, b), c, 0, 0, 0);
}
__device__ __forceinline__ v2u ldtr(lptr p) {
    return __builtin_bit_cast(v2u, __builtin_amdgcn_ds_read_tr16_b64_v4i16((LAS s16x4*)p));
}
template <typename T> __device__ __forceinline__ T lld(lptr p) { return *(LAS T*)p; }
template <typename T> __device__ __forceinline__ void lst(lptr p, T v) { *(LAS T*)p = v; }
__device__ __forceinline__ float sigm(float s) { return __builtin_amdgcn_rcpf(1.f + __expf(-s)); }

__device__ __forceinline__ u32 umax(u32 a, u32 b) { return a > b ? a : b; }
__device__ __forceinline__ u32 umin(u32 a, u32 b) { return a < b ? a : b; }
__device__ __forceinline__ u32 wave_max(u32 v) {
    v = umax(v, (u32)__builtin_amdgcn_update_dpp(0, (int)v, 0x111, 0xf, 0xf, false));
    v = umax(v, (u32)__builtin_amdgcn_update_dpp(0, (int)v, 0x112, 0xf, 0xf, false));
    v = umax(v, (u32)__builtin_amdgcn_update_dpp(0, (int)v, 0x114, 0xf, 0xf, false));
    v = umax(v, (u32)__builtin_amdgcn_update_dpp(0, (int)v, 0x118, 0xf, 0xf, false));
    v = umax(v, (u32)__builtin_amdgcn_update_dpp(0, (int)v, 0x142, 0xa, 0xf, false));
    v = umax(v, (u32)__builtin_amdgcn_update_dpp(0, (int)v, 0x143, 0xc, 0xf, false));
    return (u32)__builtin_amdgcn_readlane((int)v, 63);
}

#define TAGOK(q) (((q).x == tag) & ((q).z == tag))
#define TAGOK3(q) (((q).x == tag) & (((q).z ^ tag) < 2048u))
#define WAITN(n) asm volatile("s_waitcnt vmcnt(%0)" :: "i"(n) : "memory")
#define WAITV(n) asm volatile("s_waitcnt vmcnt(" #n ")" ::: "memory")

#define BRKS 12,23,119,67,40
constexpr int is_brk(int n) { const int l[] = {BRKS}; for (int v : l) if (v == n) return 1; return 0; }
#define PAD_TXT(n, b) ".if %c" #b "\n\ts_branch GMX_PAD%c" #n "\n\t.p2align 12\n\t.endif\nGMX_PAD%c" #n ":\n\ts_cbranch_execz GMX_RET"
#define PAD(n) asm volatile(PAD_TXT(0, 1) :: "i"(n), "i"(is_brk(n)))
#define PADV(n, v) asm volatile(PAD_TXT(1, 2) : "+v"(v) : "i"(n), "i"(is_brk(n)))
#define COLD_BEGIN() asm volatile("GMX_COLDB:" ::: "memory")
#define COLD_END() asm volatile("GMX_COLDE:" ::: "memory")
#define NPT 32
#define PADTAB "GMX_PAD60,GMX_PAD1,GMX_PAD58,GMX_PAD40,GMX_PAD63,GMX_PAD67,GMX_PAD70,GMX_PAD87,GMX_PAD48,GMX_PAD88,GMX_PAD83,GMX_RET,GMX_RET,GMX_RET,GMX_RET,GMX_RET,GMX_PAD1,GMX_PAD5,GMX_PAD10,GMX_PAD12,GMX_PAD15,GMX_PAD17,GMX_PAD21,GMX_PAD23,GMX_PAD24,GMX_PAD104,GMX_PAD110,GMX_PAD115,GMX_PAD119,GMX_PAD141,GMX_RET,GMX_RET"
#define TOUCH_HEAD                                   \
    "s_mov_b64 s[84:85], exec\n\t"                   \
    "s_getpc_b64 s[80:81]\n"                         \
    "GMX_L%=:\n\t"                                   \
    "s_lshl_b32 s82, %0, 2\n\t"                      \
    "s_add_i32 s82, s82, GMX_TAB-GMX_L%=\n\t"        \
    "s_ashr_i32 s83, s82, 31\n\t"                    \
    "s_add_u32 s80, s80, s82\n\t"                    \
    "s_addc_u32 s81, s81, s83\n\t"                   \
    "s_mov_b64 exec, 0\n\t"                          \
    "s_swappc_b64 s[82:83], s[80:81]\n\t"            \
    "s_mov_b64 exec, s[84:85]\n\t"
#define TOUCH_CLOB "s80", "s81", "s82", "s83", "s84", "s85", "scc"
#define TOUCH(idx) asm volatile(TOUCH_HEAD :: "s"(idx) : TOUCH_CLOB)
#define TOUCH_FIRST(idx) asm volatile(                                                                                   \
    TOUCH_HEAD "s_branch GMX_END\n"                                    \
    "GMX_TAB:\n\t"                                                                                                       \
    ".irp t," PADTAB "\n\ts_branch \\t\n\t.endr\n"                                                                        \
    "GMX_RET:\n\ts_setpc_b64 s[82:83]\n"                                                                                 \
    "GMX_END:" :: "s"(idx) : TOUCH_CLOB)
template <int N, int STR>
__device__ __forceinline__ void request(v4u (&q)[N], __amdgpu_buffer_rsrc_t rs, int off) {
#pragma unroll
    for (int k = 0; k < N; k++) q[k] = __builtin_amdgcn_raw_buffer_load_b128(rs, off, k * STR, SC1);
}
template <int N, int M, int STR, bool ROWTAG, int PID>
__device__ __forceinline__ void fetch(v4u (&q)[N], v4u (&r)[M ? M : 1], __amdgpu_buffer_rsrc_t rs, int off, int off2, u32 tag) {
    bool ok = false;
#pragma unroll 1
    for (int spins = 0; spins < SPIN_CAP; ++spins) {
        PAD(PID);
        CFENCE();
        if (!ok) {
            request<N, STR>(q, rs, off);
            if (M) request<M ? M : 1, STR>(r, rs, off2);
        }
        u32 aa = ~0u, oo = 0u;
#pragma unroll
        for (int k = 0; k < N; k++) {
            const u32 z = ROWTAG ? ((q[k].z & ~2047u) | (tag & 2047u)) : q[k].z;
            aa &= q[k].x & z; oo |= q[k].x | z;
        }
#pragma unroll
        for (int k = 0; k < M; k++) { aa &= r[k].x & r[k].z; oo |= r[k].x | r[k].z; }
        ok = (aa == tag) & (oo == tag);
        if (__all(ok)) break;
        if (PSLEEP) __builtin_amdgcn_s_sleep(PSLEEP);
    }
}
__device__ __forceinline__ void canary(__amdgpu_buffer_rsrc_t rs, int off, u32 tag) {
#pragma unroll 1
    for (int spins = 0; spins < SPIN_CAP; ++spins) {
        CFENCE();
        if (__all(__builtin_amdgcn_raw_buffer_load_b32(rs, off, 0, SC1) == tag)) break;
        __builtin_amdgcn_s_sleep(CSLEEP);
    }
    CFENCE();
}
__device__ __forceinline__ u32 umax3(u32 a, u32 b, u32 c) { u32 r; asm("v_max3_u32 %0, %1, %2, %3" : "=v"(r) : "v"(a), "v"(b), "v"(c)); return r; }
__device__ __forceinline__ u32 umed3(u32 a, u32 b, u32 c) { u32 r; asm("v_med3_u32 %0, %1, %2, %3" : "=v"(r) : "v"(a), "v"(b), "v"(c)); return r; }
#define DPP(v, ctl) ((u32)__builtin_amdgcn_update_dpp(0, (int)(v), ctl, 0xf, 0xf, true))
__device__ __forceinline__ u32 top8_fast(const u32 (&key)[32], int lane, u32& second) {
    const u32 lb = (u32)(63 - lane) << 5;
    u32 a1 = umax(key[0], key[1]), a2 = umin(key[0], key[1]), b1 = umax(key[2], key[3]), b2 = umin(key[2], key[3]);
#pragma unroll
    for (int k = 4; k < 32; k += 4) {
        if (k == 16) PADV(66, a1);
        a2 = umax(a2, umed3(a1, key[k], key[k + 1])); a1 = umax3(a1, key[k], key[k + 1]);
        b2 = umax(b2, umed3(b1, key[k + 2], key[k + 3])); b1 = umax3(b1, key[k + 2], key[k + 3]);
    }
    u32 a = umax(a1, b1) | lb, b = umax3(umin(a1, b1), a2, b2) | lb, t;
    second = b;
    PADV(67, a);
    const u32 c1 = (lane & 1) ? 0u : ~0u, c2 = (lane & 2) ? 0u : ~0u;
#define FLIP(ctl, c) t = umed3(a, DPP(b, ctl), c); b = umed3(b, DPP(a, ctl), c); a = t
#define HALF(ctl, c) t = umed3(a, DPP(a, ctl), c); b = umed3(b, DPP(b, ctl), c); a = t
#define INTRA() t = umax(a, b); b = umin(a, b); a = t
#define RESORT() HALF(0x4e, c2); HALF(0xb1, c1); INTRA()
    FLIP(0xb1, c1); INTRA();
    FLIP(0x1b, c2); HALF(0xb1, c1); INTRA();
    t = umax(a, DPP(b, 0x141)); b = umax(b, DPP(a, 0x141)); a = t; RESORT();
    t = umax(a, DPP(b, 0x140)); b = umax(b, DPP(a, 0x140)); a = t; RESORT();
    {
        const auto sa = __builtin_amdgcn_permlane16_swap(a, a, false, false), sb = __builtin_amdgcn_permlane16_swap(b, b, false, false);
        a = umax(sa[0], DPP(sb[1], 0x1b)); b = umax(sb[0], DPP(sa[1], 0x1b)); RESORT();
    }
    {
        const auto sa = __builtin_amdgcn_permlane32_swap(a, a, false, false), sb = __builtin_amdgcn_permlane32_swap(b, b, false, false);
        a = umax(sa[0], DPP(sb[1], 0x1b)); b = umax(sb[0], DPP(sa[1], 0x1b));
    }
    return (lane & 4) ? b : a;
}
__device__ __forceinline__ u32 top8_slow(const u32 (&key)[32], int lane) {
    const u32 lb = (u32)(63 - lane) << 5;
    u32 M = 0xffffffffu, mine = 0;
#pragma unroll 1
    for (int r = 0; r < 8; r++) {
        u32 m = 0;
#pragma unroll
        for (int k = 0; k < 32; k += 2) { const u32 v = key[k] | lb, w = key[k + 1] | lb; m = umax3(m, v < M ? v : 0u, w < M ? w : 0u); }
        M = wave_max(m);
        mine = ((lane & 7) == r) ? M : mine;
    }
    return mine;
}

struct c2 { f32x2 r, i; };
__device__ __forceinline__ c2 cmul(c2 a, c2 b) { return c2{a.r * b.r - a.i * b.i, a.r * b.i + a.i * b.r}; }
__device__ __forceinline__ c2 csq(c2 a) { const f32x2 t = a.r * a.i; return c2{a.r * a.r - a.i * a.i, t + t}; }
__device__ __forceinline__ c2 cpow(f32x2 l2, f32x2 rev) {
    c2 z;
#pragma unroll
    for (int e = 0; e < 2; e++) {
        const float m = __builtin_amdgcn_exp2f(l2[e]);
        const float f = __builtin_amdgcn_fractf(rev[e]);
        z.r[e] = m * __builtin_amdgcn_cosf(f);
        z.i[e] = m * __builtin_amdgcn_sinf(f);
    }
    return z;
}
typedef _Float16 h2_t __attribute__((ext_vector_type(2)));
typedef _Float16 h8_t __attribute__((ext_vector_type(8)));
__device__ __forceinline__ u32 pkh(float lo, float hi) { f32x2 v = {lo, hi}; return __builtin_bit_cast(u32, __builtin_convertvector(v, h2_t)); }
__device__ __forceinline__ v4u cmulh4(v4u a, v4u b) {
    u32 r0, r1, r2, r3;
    asm("v_pk_mul_f16 %0, %4, %8 op_sel_hi:[0,1]\n\tv_pk_mul_f16 %1, %5, %9 op_sel_hi:[0,1]\n\t"
        "v_pk_mul_f16 %2, %6, %10 op_sel_hi:[0,1]\n\tv_pk_mul_f16 %3, %7, %11 op_sel_hi:[0,1]\n\t"
        "v_pk_fma_f16 %0, %4, %8, %0 op_sel:[1,1,0] op_sel_hi:[1,0,1] neg_lo:[0,1,0]\n\tv_pk_fma_f16 %1, %5, %9, %1 op_sel:[1,1,0] op_sel_hi:[1,0,1] neg_lo:[0,1,0]\n\t"
        "v_pk_fma_f16 %2, %6, %10, %2 op_sel:[1,1,0] op_sel_hi:[1,0,1] neg_lo:[0,1,0]\n\tv_pk_fma_f16 %3, %7, %11, %3 op_sel:[1,1,0] op_sel_hi:[1,0,1] neg_lo:[0,1,0]"
        : "=&v"(r0), "=&v"(r1), "=&v"(r2), "=&v"(r3) : "v"(a[0]), "v"(a[1]), "v"(a[2]), "v"(a[3]), "v"(b[0]), "v"(b[1]), "v"(b[2]), "v"(b[3]));
    return (v4u){r0, r1, r2, r3};
}
__device__ __forceinline__ v2u cpk(c2 a) { return (v2u){pkh(a.r[0], a.i[0]), pkh(a.r[1], a.i[1])}; }
__device__ __forceinline__ void tr8(v4u (&o)[8], const v4u (&in)[8]) {
#pragma unroll
    for (int e = 0; e < 8; e++)
#pragma unroll
        for (int p = 0; p < 4; p++) o[e][p] = __builtin_amdgcn_perm(in[2 * p + 1][e >> 1], in[2 * p][e >> 1], (e & 1) ? 0x07060302u : 0x05040100u);
}
struct MaskTab { u32 v[6][64][4]; };
constexpr MaskTab make_masks() {
    MaskTab t{};
    const int lo[5] = {3, 6, 5, 4, 3}, hi[5] = {5, 10, 11, 12, 4};
    for (int m = 0; m < 5; m++)
        for (int l = 0; l < 64; l++)
            for (int j = 0; j < 8; j++) {
                const int xx = (l & 15) + 16 - 8 * (l >> 4) - j;
                if (xx >= lo[m] && xx <= hi[m]) t.v[m][l][j >> 1] |= (m < 4 ? 0x3f80u : 0xffffu) << (16 * (j & 1));
            }
    return t;
}
__device__ const MaskTab g_masks = make_masks();
__device__ __forceinline__ void dma16(const void* gsrc, u32 lds_dst) {
    asm volatile("s_mov_b32 m0, %1\n\ts_nop 0\n\tglobal_load_lds_dwordx4 %0, off nt" :: "v"(gsrc), "s"(lds_dst) : "memory");
}
__device__ __forceinline__ void dma16c(const void* gsrc, u32 lds_dst) {
    asm volatile("s_mov_b32 m0, %1\n\ts_nop 0\n\tglobal_load_lds_dwordx4 %0, off" :: "v"(gsrc), "s"(lds_dst) : "memory");
}
__device__ __forceinline__ void dma4(const void* gsrc, u32 lds_dst) {
    asm volatile("s_mov_b32 m0, %1\n\ts_nop 0\n\tglobal_load_lds_dword %0, off" :: "v"(gsrc), "s"(lds_dst) : "memory");
}
#define SETF(f, v) do { CFENCE(); lst<u32>(lds + FL_OFF + (f) * 4, (u32)(v)); CFENCE(); } while (0)
__device__ __forceinline__ void waitf(lptr a, u32 v) {
#pragma unroll 1
    for (int spins = 0; spins < LSPIN_CAP; ++spins) {
        CFENCE();
        if (__all(lld<u32>(a) >= v)) break;
        __builtin_amdgcn_s_sleep(1);
    }
    CFENCE();
}
#define WAITF(f, v) waitf(lds + FL_OFF + (f) * 4, v)
__device__ __forceinline__ void l2pull(const void* gsrc, u32 lds_dst) {
    asm volatile("s_mov_b32 m0, %1\n\ts_nop 0\n\tglobal_load_lds_dword %0, off nt\n\tglobal_load_lds_dword %0, off offset:64" :: "v"(gsrc), "s"(lds_dst) : "memory");
}
#define GC(p) ((const char*)(p))
#define MKRS() __builtin_amdgcn_make_buffer_rsrc(ws, 0, WS_USED, 0x00020000)
#define MKTAG(e) (((e) + 1u) * 0x9E3779B1u ^ 0x5bd1e995u)
#define EPOCH() (*(const __attribute__((address_space(4))) u32*)(ws + OFF_E))

__global__ void __launch_bounds__(512, 2) __attribute__((aligned(4096)))
fused_kernel(const u16* __restrict__ x, const u16* __restrict__ wr, const u16* __restrict__ gw, const u16* __restrict__ logdt,
             const u16* __restrict__ logA, const u16* __restrict__ Aim, const u16* __restrict__ Cre, const u16* __restrict__ Cim,
             char* ws, const u16* __restrict__ dskip, const u16* __restrict__ wg, const u16* __restrict__ wu,
             const u16* __restrict__ wd, u16* __restrict__ out) {
    __shared__ __attribute__((aligned(16))) char smem[LDS_BYTES];
    const lptr lds = (lptr)smem;
    const int tid = threadIdx.x, lane = tid & 63;
    STAMP(0);
    const u32 tstart = (u32)__builtin_amdgcn_s_memrealtime();
    const int wave = __builtin_amdgcn_readfirstlane(tid >> 6);
    const u32 lds0 = (u32)(uintptr_t)lds;
    if (lane < 8) {
        const u32 ln = (((blockIdx.x >> 3) & 3) * 64 + wave * 8 + lane);
        dma4((const char*)(__builtin_amdgcn_s_getpc() & ~4095ull) + (ln < CODE_LINES ? ln : 0u) * 128, lds0 + DUMP_OFF);
    }
    if (tid < 32) lst<u32>(lds + FL_OFF + tid * 4, 0u);
    int role = (blockIdx.x >> 5) & 1;
    const int i = lane & 15, g = lane >> 4;
    const int xcd = blockIdx.x & 7, jx = ((blockIdx.x >> 3) & 3) | ((blockIdx.x >> 6) << 2);
    const int pb = xcd * 16 + jx;
    const int ct = SAMEX ? (xcd * 14 + jx) >> 2 : (xcd >> 2) * 14 + jx, kq = SAMEX ? (xcd * 14 + jx) & 3 : xcd & 3;
    const bool has_w = jx < 14;
    asm volatile("" :: "s"(x), "s"(wr), "s"(gw), "s"(logdt), "s"(logA), "s"(Aim), "s"(Cre), "s"(Cim), "s"(ws), "s"(dskip), "s"(wg), "s"(wu));
    u32 tag0 = 0;
    if (!role) {
        BARRIER();
        if ((u32)(wave - 2) < 4u) {
#pragma unroll 1
            for (int t = wave - 2; t < NPT / 2; t += 4) TOUCH(t);
        } else {
            const int hf = wave & 1, bk = wave >> 2;
            v4u xa[8], wb[2];
            const char* vrow = GC((i & 1) ? gw : wr) + (u32)(1024 * hf + 64 * (i >> 1) + 16 * g);
            wb[0] = *(const v4u*)vrow; wb[1] = *(const v4u*)(vrow + 512);
#pragma unroll
            for (int p = 0; p < 8; p++) xa[p] = XLD(v4u, GC(x) + (u32)((2 * pb + bk) * 16384 + (i & 7) * 2048 + 1024 * hf + (i >> 3) * 64 + g * 16) + 128 * p);
            SCHED();
            tag0 = MKTAG(EPOCH());
            asm volatile("" : "+s"(tag0));
            SCHED();
            PAD(60);
            f32x4 ac[4], v;
#pragma unroll
            for (int p = 0; p < 8; p++) ac[p & 3] = mfma16(xa[p], wb[p >> 2], p < 4 ? (f32x4){0, 0, 0, 0} : ac[p & 3]);
#pragma unroll
            for (int r = 0; r < 4; r++) v[r] = (i & 8) ? ((i & 4) ? ac[3][r] : ac[2][r]) : ((i & 4) ? ac[1][r] : ac[0][r]);
            if (((i >> 1) & 1) == (g >> 1)) lst<f32x4>(lds + PD_OFF + (((i & 1) * 4 + bk * 2 + (g & 1)) * 17 + hf * 8 + (i >> 1)) * 16, v);
            SETF(F_PD + (wave & 3), 1);
        }
    }
    asm volatile("" : "+s"(role));
    if (role) {
        dma16(GC(g == 0 ? logA : g == 1 ? Aim : g == 2 ? Cre : Cim) + (u32)((8 * pb + wave) * 256 + i * 16), lds0 + PRM_OFF + wave * 1280);
        dma4(g == 0 ? GC(logdt) + (pb * 16 + (wave >> 1) * 4) : g == 1 ? GC(dskip) + (pb * 16 + (wave >> 1) * 4) : GC(ws) + OFF_E, lds0 + PRM_OFF + wave * 1280 + 1024);
        BARRIER();
        if (wave < 4) {
            dma16c(GC(&g_masks) + wave * 1024 + lane * 16, lds0 + MSK_OFF + wave * 1024);
            dma16c(GC(&g_masks) + 4096 + lane * 16, lds0 + MSK_OFF + 4096);
        } else if (EARLYPULL) {
            l2pull(GC(x) + (u32)((jx * 128 + (wave & 3) * 32 + (lane >> 1)) * 2048 + xcd * 256 + (lane & 1) * 128), lds0 + DUMP_OFF);
        }
        TOUCH_FIRST(NPT / 2 + wave);
        if (wave >= 4) { TOUCH(NPT / 2 + 4 + wave); TOUCH(NPT / 2 + 8 + wave); }
    }
    asm volatile("" : "+s"(role));

    if (!role) {
        PAD(58);
        const __amdgpu_buffer_rsrc_t rs = MKRS();
        const int lw = wave - 2;
        u32 tag = tag0;
        if ((u32)lw < 4u) {
            PAD(40);
            if (!has_w) return;
            {
                if (WDELAY) __builtin_amdgcn_s_sleep(WDELAY);
                const char* wp = GC(lane < 32 ? wg : wu) + (u32)((256 * kq + 64 * lw + (g & 1)) * 7168 + 256 * ct);
                const u32 c0 = (u32)(i ^ ((g & 1) << 1));
#pragma unroll 1
                for (int j = 0; j < NGU; j++) {
                    if (j == DAT) SETF(F_GU + lw, 1);
                    WAITN(WIN - 1);
                    const int jb = (j & 16) | ((j * (PERM ? 2 * lw + 1 + 8 * (jx & 1) : 1) + (PERM ? 5 * jx : 0)) & 15);
                    const int jm = jb ^ (PERM > 1 && lane >= 32 ? 10 : 0);
                    dma16(wp + (u32)(jm * 2 * 7168) + ((c0 ^ (u32)(((jb & 1) << 2) | ((jb & 4) << 1))) << 4), lds0 + GU_OFF + lw * 32768 + jb * 1024);
                }
            }
            STAMP(1);
            __builtin_amdgcn_s_setprio(3);
            f32x4 acc[16];
            v4u qg[3], qu[3];
            const int grp = OFF_H2 + ct * 7 * 8192 + lw * 2048 + lane * 16;
            {
                const lptr gp = lds + GU_OFF + lw * 32768 + g * 4096 + (i >> 3) * 1024 + ((i >> 2) & 1) * 256 + (i & 3) * 8;
                const int sw = (i >> 2) | ((g & 1) << 2);
                v4u wf[4][8];
#pragma unroll
                for (int u = 0; u < 4; u++) {
                    PAD(42 + u);
                    if (u == 2) { WAITV(0); STAMP(2); }
#pragma unroll
                    for (int e = 0; e < 8; e++) {
                        const lptr q = gp + (u >> 1) * 16384 + (u & 1) * 512 + ((e ^ sw) << 5);
                        const v2u lo = ldtr(q), hh = ldtr(q + 2048);
                        wf[u][e] = (v4u){lo.x, lo.y, hh.x, hh.y};
                    }
                }
                WAITF(F_XS, 1);
                PAD(46);
                if (EARLYH2) { request<3, 8192>(qg, rs, grp + (kq + 1) * 8192); request<3, 8192>(qu, rs, grp + (kq + 1) * 8192 + 1024); SCHED(); }
                const v4u xb0 = lld<v4u>(lds + XS_OFF + 2 * lw * 512 + g * 128 + (i & 7) * 16), xb1 = lld<v4u>(lds + XS_OFF + (2 * lw + 1) * 512 + g * 128 + (i & 7) * 16);
#pragma unroll
                for (int u = 0; u < 4; u++)
#pragma unroll
                    for (int e = 0; e < 8; e++) acc[8 * (u & 1) + e] = mfma16(wf[u][e], u >= 2 ? xb1 : xb0, u >= 2 ? acc[8 * (u & 1) + e] : (f32x4){0, 0, 0, 0});
            }
            PAD(48);
            tag = __builtin_amdgcn_readfirstlane(lld<u32>(lds + FL_OFF + F_TAG * 4));
            {
                const int hi = i >> 3;
                const lptr pp = lds + GU_OFF + lw * 32768 + (hi * 8 + (i & 7)) * 528 + g * 16;
#pragma unroll
                for (int e = 0; e < 8; e++) {
                    f32x4 v;
#pragma unroll
                    for (int r = 0; r < 4; r++) v[r] = hi ? acc[8 + e][r] : acc[e][r];
                    lst<f32x4>(pp + e * 64, v);
                }
            }
            SETF(F_PP + lw, 1);
            waitf(lds + FL_OFF + (F_PP + (lane & 3)) * 4, 1);
            STAMP(6);
            PAD(80);
            const int tok = 2 * lw + (lane >> 5), cq = lane & 31;
            f32x4 G = {0, 0, 0, 0}, U = {0, 0, 0, 0};
#pragma unroll
            for (int w = 0; w < 4; w++) {
                G += lld<f32x4>(lds + GU_OFF + w * 32768 + tok * 528 + cq * 16);
                U += lld<f32x4>(lds + GU_OFF + w * 32768 + (8 + tok) * 528 + cq * 16);
            }
            {
                const v4u og = {tag, pk(G[0], G[1]), tag, pk(G[2], G[3])}, ou = {tag, pk(U[0], U[1]), tag, pk(U[2], U[3])};
                if (kq > 0) { __builtin_amdgcn_raw_buffer_store_b128(og, rs, grp + kq * 8192, 0, SC1); __builtin_amdgcn_raw_buffer_store_b128(ou, rs, grp + kq * 8192 + 1024, 0, SC1); }
                if (kq < 3) { __builtin_amdgcn_raw_buffer_store_b128(og, rs, grp + (kq + 4) * 8192, 0, SC1); __builtin_amdgcn_raw_buffer_store_b128(ou, rs, grp + (kq + 4) * 8192 + 1024, 0, SC1); }
            }
            SETF(F_GU + lw, 2);
            STAMP(7);
            PAD(82);
            bool have = false;
            if (EARLYH2) {
                u32 aa = ~0u, oo = 0u;
#pragma unroll
                for (int k = 0; k < 3; k++) { aa &= qg[k].x & qg[k].z & qu[k].x & qu[k].z; oo |= qg[k].x | qg[k].z | qu[k].x | qu[k].z; }
                have = __all((aa == tag) & (oo == tag));
            }
            if (!have) {
                if (H2ACK) WAITV(0);
                fetch<3, 3, 8192, false, 83>(qg, qu, rs, grp + (kq + 1) * 8192, grp + (kq + 1) * 8192 + 1024, tag);
            }
#pragma unroll
            for (int k = 0; k < 3; k++) {
                G += (f32x4){bflo(qg[k].y), bfhi(qg[k].y), bflo(qg[k].w), bfhi(qg[k].w)};
                U += (f32x4){bflo(qu[k].y), bfhi(qu[k].y), bflo(qu[k].w), bfhi(qu[k].w)};
            }
            PADV(85, G);
            float hv[4];
#pragma unroll
            for (int r = 0; r < 4; r++) { const float gr = rbf(G[r]); hv[r] = rbf(gr * sigm(gr)) * rbf(U[r]); }
            lst<v2u>(lds + HS_OFF + tok * 272 + cq * 8, (v2u){pk(hv[0], hv[1]), pk(hv[2], hv[3])});
            SETF(F_H + lw, 1);
            STAMP(8);
            STAMP_OUT();
            return;
        }
        if (wave == 0) {
            __builtin_amdgcn_s_setprio(3);
            PAD(61);
            const lptr pd = lds + PD_OFF + ((lane >> 3) * 17 + (lane & 7)) * 16;
            waitf(lds + FL_OFF + (F_PD + (lane & 3)) * 4, 1);
            f32x4 s = lld<f32x4>(pd) + lld<f32x4>(pd + 128);
#pragma unroll
            for (int r = 0; r < 4; r++) {
                float v = s[r];
                v += __uint_as_float(DPP(__float_as_uint(v), 0xb1)); v += __uint_as_float(DPP(__float_as_uint(v), 0x4e));
                v += __uint_as_float(DPP(__float_as_uint(v), 0x141));
                s[r] = v;
            }
            const u32 p01 = pk(s[0], s[1]), p23 = pk(s[2], s[3]);
            const int rq = 4 * pb + ((lane >> 3) & 3);
            if ((lane & 38) == 0) {
                v4u o;
                o.x = tag; o.z = tag;
                o.y = p01 ^ (0x80008000u | (((p01 >> 15) & 0x10001u) * 0x7fffu));
                o.w = p23 ^ (0x80008000u | (((p23 >> 15) & 0x10001u) * 0x7fffu));
                __builtin_amdgcn_raw_buffer_store_b128(o, rs, OFF_R + (lane & 1) * 8192 + (rq >> 3) * 16 + (rq & 7) * 1024, 0, SC1);
            }
            STAMP(1);
            SCHED();
            if ((lane & 38) == 32) {
                v4u go;
                go.x = tag; go.z = tag;
                go.y = pk(sigm(bflo(p01)), sigm(bfhi(p01)));
                go.w = pk(sigm(bflo(p23)), sigm(bfhi(p23)));
                __builtin_amdgcn_raw_buffer_store_b128(go, rs, OFF_G + (lane & 1) * 8192 + rq * 16, 0, SC1);
            }
            if (!has_w && pb != 127) return;
            PAD(62);
            if (RDELAY) __builtin_amdgcn_s_sleep(RDELAY);
            if (RCAN) WAITV(0);
            if (RCAN > 1) canary(rs, OFF_R + (xcd & 1) * 8192 + (lane & 7) * 1024 + ((pb + 37) & 63) * 16, tag);
            STAMP(2);
            v4u q[8], nil[1];
            fetch<8, 0, 1024, false, 63>(q, nil, rs, OFF_R + (xcd & 1) * 8192 + lane * 16, 0, tag);
            if (__builtin_expect(!has_w, 0)) {
#pragma unroll 1
                for (int spins = 0; spins < SPIN_CAP; ++spins) {
                    __builtin_amdgcn_s_sleep(32);
                    CFENCE();
                    const v4u q0 = __builtin_amdgcn_raw_buffer_load_b128(rs, OFF_FL + (lane & 31) * 16, 0, SC1);
                    if (__all((q0.x == tag) & (q0.y == tag) & (q0.z == tag) & (q0.w == tag))) break;
                }
                CFENCE();
                if (lane == 0) __builtin_amdgcn_raw_buffer_store_b32(EPOCH() + 1u, rs, OFF_E, 0, SC1);
                return;
            }
            STAMP(3); STAMPC(12);
            PAD(65);
            u32 key[32], second;
#pragma unroll
            for (int k = 0; k < 8; k++) {
                key[4 * k] = (q[k].y << 16) | (u32)(31 - 4 * k); key[4 * k + 1] = (q[k].y & 0xffff0000u) | (u32)(30 - 4 * k);
                key[4 * k + 2] = (q[k].w << 16) | (u32)(29 - 4 * k); key[4 * k + 3] = (q[k].w & 0xffff0000u) | (u32)(28 - 4 * k);
            }
            u32 mine = top8_fast(key, lane, second);
            STAMP(4); STAMPC(13);
            PADV(69, mine);
            const u32 co = (u32)(512 * kq + 64 * (i >> 3) + 16 * g);
            v4u xv[4];
#pragma unroll
            for (int p = 0; p < 4; p++) xv[p] = *(const v4u*)(GC(x) + ((2047u - (mine & 2047u)) * 2048u + co) + 128 * p);
            SCHED();
            {
                u32 k8 = umin(mine, DPP(mine, 0xb1)); k8 = umin(k8, DPP(k8, 0x4e)); k8 = umin(k8, DPP(k8, 0x141));
                if (__any(second >= k8)) {
                    const u32 thr = __builtin_elementwise_sub_sat(k8, (u32)(63 - lane) << 5);
                    u32 cnt = 0;
#pragma unroll
                    for (int k = 0; k < 32; k++) cnt += key[k] >= thr;
                    if (__builtin_expect(__any(cnt > 2u), 0)) {
                        COLD_BEGIN();
                        mine = top8_slow(key, lane);
#pragma unroll
                        for (int p = 0; p < 4; p++) xv[p] = *(const v4u*)(GC(x) + ((2047u - (mine & 2047u)) * 2048u + co) + 128 * p);
                        COLD_END();
                    }
                }
            }
            SCHED();
            PAD(70);
            if (lane < 8) lst<u32>(lds + KEY_OFF + lane * 4, mine);
            lst<u32>(lds + FL_OFF + F_TAG * 4, tag);
#pragma unroll
            for (int p = 0; p < 4; p++) lst<v4u>(lds + XS_OFF + (2 * p + (i >> 3)) * 512 + g * 128 + (i & 7) * 16, xv[p]);
            SETF(F_XS, 1);
            STAMP(5);
        } else {
            if (!has_w) return;
        }
        PAD(41);
        const int un = wave & 3;
        v2u din[32];
        const u32 doff = (u32)((128 * ct + 8 * g) * 2048 + 512 * kq + 128 * un + 8 * i);
        waitf(lds + FL_OFF + (F_GU + (lane & 3)) * 4, DGATE);
        if (DTIME) { while ((u32)__builtin_amdgcn_s_memrealtime() - tstart < (u32)DTIME) __builtin_amdgcn_s_sleep(2); }
#pragma unroll
        for (int p = 0; p < 32; p++) { if (p >= DWIN) WAITN(DWIN - 1); din[p] = WLD(v2u, GC(wd) + (doff + (p >> 3) * 65536) + (p & 7) * 2048); }
        SCHED();
        __builtin_amdgcn_s_setprio(3);
        v4u dfr[4][4];
#pragma unroll
        for (int s = 0; s < 4; s++) {
#pragma unroll
            for (int e = 0; e < 4; e++)
#pragma unroll
                for (int p = 0; p < 4; p++)
                    dfr[s][e][p] = __builtin_amdgcn_perm(din[8 * s + 2 * p + 1][e >> 1], din[8 * s + 2 * p][e >> 1], (e & 1) ? 0x07060302u : 0x05040100u);
            if (s & 1) PADV(86 + (s >> 1), dfr[s][3]);
        }
        WAITF(F_XS, 1);
        tag = __builtin_amdgcn_readfirstlane(lld<u32>(lds + FL_OFF + F_TAG * 4));
        const u32 key = lld<u32>(lds + KEY_OFF + (i & 7) * 4), sk = key >> 16;
        float rsel = rbf(sigm(__uint_as_float(((sk & 0x8000u) ? (sk ^ 0x8000u) : (sk ^ 0xffffu)) << 16)));
        u32 tagrow = tag ^ (2047u - (key & 2047u));
        const int hi = i >> 3;
        int ho = OFF_H3 + ((32 * kq + 8 * un + 2 * g + hi) * 28 + ct) * 256 + (i & 7) * 16;
#pragma unroll
        for (int s = 0; s < 4; s++)
#pragma unroll
            for (int e = 0; e < 4; e++) asm volatile("" : "+v"(dfr[s][e]));
        asm volatile("" : "+v"(rsel), "+v"(tagrow), "+v"(ho));
        STAMP(7);
        waitf(lds + FL_OFF + (F_H + (lane & 3)) * 4, 1);
        STAMP(8);
        PAD(88);
        {
            f32x4 acc[4];
            v4u hb[4];
#pragma unroll
            for (int s = 0; s < 4; s++) hb[s] = lld<v4u>(lds + HS_OFF + (i & 7) * 272 + 64 * s + 16 * g);
#pragma unroll
            for (int s = 0; s < 4; s++)
#pragma unroll
                for (int e = 0; e < 4; e++) acc[e] = mfma16(dfr[s][e], hb[s], s ? acc[e] : (f32x4){0, 0, 0, 0});
#pragma unroll
            for (int r = 0; r < 2; r++) {
                v4u o;
                o.x = tag; o.z = tagrow;
                o.y = pk((hi ? acc[0][2 + r] : acc[0][r]) * rsel, (hi ? acc[1][2 + r] : acc[1][r]) * rsel);
                o.w = pk((hi ? acc[2][2 + r] : acc[2][r]) * rsel, (hi ? acc[3][2 + r] : acc[3][r]) * rsel);
                __builtin_amdgcn_raw_buffer_store_b128(o, rs, ho + r * 128, 0, SC1);
            }
        }
        STAMP(9);
        STAMP_OUT();
        return;
    }

    PAD(0);
    const lptr wl = lds + wave * WR;
    const __amdgpu_buffer_rsrc_t rs = MKRS();
    lst<v2u>(wl + S_OFF + lane * 8, (v2u){0, 0});
    PAD(1);
    const int r32 = lane & 31, h = lane >> 5;
    lptr ea_, pa, qa;
    {
        const u64 t1 = 0x0d0c0b080b0a0904ull, t2 = 0x0f0e151413121110ull;
        const int erow = (r32 < 8 || r32 >= 24) ? (r32 & 7) : (int)(((r32 < 16 ? t1 : t2) >> (8 * (r32 & 7))) & 31u);
        ea_ = wl + erow * ROWB + h * 16;
        pa = wl + (r32 >> 2) * ROWB + h * 16; qa = wl + ((r32 & 3) ? 7 + (r32 & 3) : 0) * ROWB + h * 16;
        asm volatile("" : "+v"(ea_), "+v"(pa), "+v"(qa));
    }
    const char* pl = GC(x) + (u32)((jx * 128 + (wave & 3) * 32 + (lane >> 1)) * 2048 + xcd * 256 + (lane & 1) * 128);
    asm volatile("" : "+v"(pl));
    PAD(4);
    if (BPRIO) { if (wave >= 4) __builtin_amdgcn_s_setprio(1); else __builtin_amdgcn_s_setprio(2); }
    if (EARLYPULL || wave < 4) WAITV(2); else WAITV(0);
    if (!EARLYPULL && wave < 4) l2pull(pl, lds0 + DUMP_OFF);
    STAMP(1);
    const lptr prm = lds + PRM_OFF + wave * 1280;
    const u32 pla = lld<u32>(prm + lane * 4), pai = lld<u32>(prm + 256 + lane * 4);
    const u32 pcr = lld<u32>(prm + 512 + lane * 4), pci = lld<u32>(prm + 768 + lane * 4);
    const u32 sld = lld<u32>(prm + 1024), sds = lld<u32>(prm + 1024 + 64);
    const u32 tag = MKTAG(__builtin_amdgcn_readfirstlane(lld<u32>(prm + 1024 + 128)));
    v4u xc[4], gq[1], nil[1];
    const float ldt = (wave & 1) ? bfhi(sld) : bflo(sld);
    float dm1 = ((wave & 1) ? bfhi(sds) : bflo(sds)) - 1.f;
    {
        const float dt = rbf(__expf(ldt));
        const u32 ea = pk(__expf(bflo(pla)), __expf(bfhi(pla)));
        const f32x2 l2 = (f32x2){bflo(ea) * dt, bfhi(ea) * dt} * -1.4426950408889634f;
        const f32x2 rev = (f32x2){bflo(pai) * dt, bfhi(pai) * dt} * -0.15915494309189535f;
        const u32 cm = umax(pcr & 0x7f807f80u, pci & 0x7f807f80u), cl = umax((pcr << 16) & 0x7f800000u, (pci << 16) & 0x7f800000u);
        const u32 ebits = umin(wave_max(umax(cm, cl)) & 0x7f800000u, 0x7e800000u);
        const float cinv = __uint_as_float(0x7f000000u - ebits), kscale = 2.f * dt * __uint_as_float(ebits);
        const c2 z1 = cpow(l2, rev), zB = cpow(l2 * 256.f, rev * 256.f);
        const c2 z2 = csq(z1), z4 = csq(z2);
        PAD(5);
        const lptr wp = wl + lane * 8;
#define PUTP(row, pv) lst<v2u>(wp + (row) * ROWB, pv)
#define PUT(row, v) er[row] = cpk(v)
        v2u er[22];
        c2 e[9];
        e[0] = c2{(f32x2){bflo(pcr), bfhi(pcr)} * cinv, (f32x2){bflo(pci), bfhi(pci)} * -cinv};
        e[4] = cmul(e[0], z4); e[8] = cmul(e[4], z4);
        e[2] = cmul(e[0], z2); e[6] = cmul(e[4], z2);
        c2 t8 = e[8], t4c = e[4], q1, q2, p1, p2, p3, p4;
        v2u pq[11];
#pragma unroll
        for (int st = 0; st < 7; st++) {
            PADV(6 + st, t8.r);
            t8 = cmul(t8, zB); PUT(st == 0 ? 13 : st == 1 ? 17 : st == 2 ? 21 : st < 5 ? 15 + st : 9 + st, t8);
            if (st < 3) { t4c = cmul(t4c, zB); PUT(st == 0 ? 11 : st == 1 ? 16 : 20, t4c); }
            if (st >= 3) { PUT(2 * (st - 3), e[2 * (st - 3)]); PUT(2 * (st - 3) + 1, cmul(e[2 * (st - 3)], z1)); }
            if (st == 0) { PUT(8, e[8]); PUT(9, cmul(e[0], zB)); }
            if (st == 1) PUT(10, cmul(e[2], zB));
            if (st == 2) PUT(12, cmul(e[6], zB));
            if (st == 0) { q1 = csq(z4); q1.i = -q1.i; pq[8] = cpk(q1); }
            if (st == 1) { q2 = csq(q1); pq[9] = cpk(q2); }
            if (st == 2) { p1 = csq(q2); pq[1] = cpk(p1); pq[10] = cpk(cmul(q2, q1)); }
            if (st == 3) { p2 = csq(p1); pq[2] = cpk(p2); }
            if (st == 4) { p3 = cmul(p2, p1); p4 = csq(p2); pq[3] = cpk(p3); pq[4] = cpk(p4); }
            if (st == 5) { pq[5] = cpk(cmul(p4, p1)); pq[6] = cpk(csq(p3)); }
            if (st == 6) pq[7] = cpk(cmul(p4, p3));
        }
        SCHED();
#pragma unroll
        for (int r = 0; r < 22; r++) PUTP(r, er[r]);
        SCHED();
        STAMP(2);
        PAD(13);
        v4u ef[16];
#pragma unroll
        for (int s = 0; s < 16; s++) ef[s] = lld<v4u>(ea_ + s * 32);
        SCHED();
        pq[0] = (v2u){0x3c00u, 0x3c00u};
#pragma unroll
        for (int r = 0; r < 11; r++) PUTP(r, pq[r]);
        lst<v4u>(wl + T_P2 + lane * 16, (v4u){0, 0, 0, 0}); lst<v4u>(wl + T_P2 + 1024 + lane * 16, (v4u){0, 0, 0, 0});
        lst<u32>(wl + T_PN + (lane < 32 ? lane : lane + 512) * 4, 0u);
        SCHED();
        STAMP(3);
        PAD(14);
        f32x16 kd = {0};
        {
            v4u pv[16], qv[16], ff[16];
#pragma unroll
            for (int s = 0; s < 4; s++) { pv[s] = lld<v4u>(pa + s * 32); qv[s] = lld<v4u>(qa + s * 32); }
#pragma unroll
            for (int s = 0; s < 16; s++) {
                if (s + 4 < 16) { pv[s + 4] = lld<v4u>(pa + (s + 4) * 32); qv[s + 4] = lld<v4u>(qa + (s + 4) * 32); }
                ff[s] = cmulh4(pv[s], qv[s]);
                SCHED();
                if (s > 0) kd = __builtin_amdgcn_mfma_f32_32x32x16_f16(__builtin_bit_cast(h8_t, ef[s - 1]), __builtin_bit_cast(h8_t, ff[s - 1]), kd, 0, 0, 0);
                if (s == 5 || s == 11) PAD(15 + s / 6);
                if ((s & 3) == XPH) xc[s >> 2] = XLD(v4u, GC(x) + (u32)(tid * 8192 + pb * 16 + 4096) + ((s >> 2) - 2) * 2048);
                SCHED();
            }
            asm volatile("s_nop 1" : "+v"(ff[15]));
            kd = __builtin_amdgcn_mfma_f32_32x32x16_f16(__builtin_bit_cast(h8_t, ef[15]), __builtin_bit_cast(h8_t, ff[15]), kd, 0, 0, 0);
        }
        STAMP(4);
        PAD(17);
        request<1, 1024>(gq, rs, OFF_G + (xcd & 1) * 8192 + tid * 16);
        STAMP(5);
#pragma unroll
        for (int r = 0; r < 12; r++) kd[r] *= kscale;
        const int b = r32;
        lst<v4u>(wl + T_PN + (544 - 8 * b - 4 * h) * 4, (v4u){pk(kd[4], kd[3]), pk(kd[3], kd[2]), pk(kd[2], kd[1]), pk(kd[1], kd[0])});
        const u32 k77 = pk(kd[7], kd[7]);
        lst<v4u>(wl + T_PN + (288 - 8 * b - 4 * h) * 4, (v4u){k77, pk(kd[7], kd[6]), pk(kd[6], kd[5]), pk(kd[5], kd[5])});
        if (lane == 0) lst<u32>(wl + T_PN + 548 * 4, pk(kd[0], 0.f));
        if (lane == 63) lst<v4u>(wl + T_PN + 32 * 4, (v4u){0u, k77 & 0xffff0000u, k77, k77});
        PAD(18);
        const u32 s2 = pk(kd[8], kd[9]), s3 = pk(kd[10], kd[11]);
        const lptr t2 = wl + T_P2 + (2 * (b & 1) * 80 + 52 - 16 * h - (b >> 1)) * 4;
        const lptr t3 = wl + T_P3 + ((b & 1) * 96 + 52 - 32 * h - (b >> 1)) * 4;
        lst<u16>(t2, (u16)s2); lst<u16>(t2 + 320, (u16)(s2 >> 16)); lst<u16>(t3 + 64, (u16)s3); lst<u16>(t3, (u16)(s3 >> 16));
        CFENCE();
        lst<u16>(t2 - 2, (u16)s2); lst<u16>(t2 + 318, (u16)(s2 >> 16)); lst<u16>(t3 + 62, (u16)s3); lst<u16>(t3 - 2, (u16)(s3 >> 16));
    }
#define UG_STORE \
_Pragma("unroll") \
    for (int e = 0; e < 8; e++) { \
        const u32 sel = (e & 1) ? 0x07060302u : 0x05040100u; \
        lst<v2u>(lds + e * WR + U_OFF + tid * 8, (v2u){__builtin_amdgcn_perm(xc[1][e >> 1], xc[0][e >> 1], sel), __builtin_amdgcn_perm(xc[3][e >> 1], xc[2][e >> 1], sel)}); \
    } \
    if (__builtin_expect(!__all(TAGOK(gq[0])), 0)) fetch<1, 0, 1024, false, 24>(gq, nil, rs, OFF_G + (xcd & 1) * 8192 + tid * 16, 0, tag); \
    lst<f32x4>(lds + G_OFF + tid * 16, (f32x4){bflo(gq[0].y), bfhi(gq[0].y), bflo(gq[0].w), bfhi(gq[0].w)}); \
    SETF(F_UG + wave, 1); \
    STAMP(7); \

    PAD(25);
    v4u kn[17], k2[3][2], k3[4], ks;
    {
        PAD(19);
        const lptr pn = wl + T_PN + (20 - i + 8 * g) * 4;
#pragma unroll
        for (int s = 0; s < 17; s++)
#pragma unroll
            for (int d = 0; d < 4; d++) kn[s][d] = lld<u32>(pn + (512 - 32 * s + 2 * d) * 4);
        PAD(20);
        const lptr p2 = wl + T_P2 + (20 - i + 4 * g) * 4;
#pragma unroll
        for (int s = 0; s < 3; s++)
#pragma unroll
            for (int rp = 0; rp < 2; rp++)
#pragma unroll
                for (int d = 0; d < 4; d++) k2[s][rp][d] = lld<u32>(p2 + ((2 * rp + (d >> 1)) * 80 + 32 - 16 * s + 2 * (d & 1)) * 4);
        PAD(21);
        const lptr p3 = wl + T_P3 + (20 - i + 4 * g) * 4;
#pragma unroll
        for (int s = 0; s < 4; s++)
#pragma unroll
            for (int d = 0; d < 4; d++) k3[s][d] = lld<u32>(p3 + ((d >> 1) * 96 + 48 - 16 * s + 2 * (d & 1)) * 4);
        ks[0] = (u32)lld<u16>(wl + T_P2 + (3 * 80 + 84 - 63) * 4) * 0x10001u;
#pragma unroll
        for (int s = 0; s < 17; s++) asm volatile("" : "+v"(kn[s]));
#pragma unroll
        for (int s = 0; s < 3; s++) asm volatile("" : "+v"(k2[s][0]), "+v"(k2[s][1]));
#pragma unroll
        for (int s = 0; s < 4; s++) asm volatile("" : "+v"(k3[s]));
    }
    lptr ub = wl + U_OFF + (16 * i + 8 * g - 16) * 2;
    lptr eb = wl + U_OFF + (16 * i + 4 * g) * 2;
    lptr sb = wl + S_OFF + 32 * i + 8 * (g ^ (i >> 2));
    lptr gb = lds + G_OFF + (16 * i + 4 * g) * 4;
    lptr pg = lds + FL_OFF + (F_PROG + wave) * 4;
    const char* hdn = GC(wd) + (u32)(((xcd >> 2) * 1792 + jx * 112 + wave * 14 + (lane < 56 ? lane >> 2 : 0)) * 2048 + (xcd & 3) * 512 + (lane & 3) * 128);
    asm volatile("" : "+v"(ub), "+v"(eb), "+v"(sb), "+v"(gb), "+v"(pg), "+v"(dm1), "+v"(hdn));
    PAD(22);
    STAMP(6);
    UG_STORE
    if (HELP == 1 || (HELP == 2 && wave < 4)) l2pull(hdn, lds0 + DUMP_OFF);
    waitf(lds + FL_OFF + (F_UG + (lane & 3)) * 4, 1);
    PAD(23);
    __builtin_amdgcn_s_setprio(0);
    v4u mk[4];
#pragma unroll
    for (int w = 0; w < 4; w++) mk[w] = lld<v4u>(lds + MSK_OFF + w * 1024 + lane * 16);
    ks = lld<v4u>(lds + MSK_OFF + 4096 + lane * 16) & ks[0];
    STAMP(8); STAMPC(12);

#define CV(d) (v2u){pk((d)[0], (d)[1]), pk((d)[2], (d)[3])}
    const f32x4 zero4 = {0, 0, 0, 0};
    f32x4 acc[8];
#pragma unroll
    for (int t = 0; t < 8; t++) acc[t] = zero4;
    v4u bc[9];
#pragma unroll
    for (int ww = 7; ww >= 0; ww--) { bc[ww] = lld<v4u>(ub + (ww - 7) * 64); if (ww == 7) bc[8] = lld<v4u>(ub - 16); }
    SCHED();
#define PIN(x) asm volatile("" : "+v"(x))
#pragma unroll
    for (int tau = 0; tau < 8; tau++) {
        PAD(100 + 3 * tau);
        const v2u uu = lld<v2u>(eb + tau * 512);
        const f32x4 gg = lld<f32x4>(gb + tau * 1024);
        f32x4 d[6];
        v2u c[6];
        float u4[4], o[4];
        const int nb = tau <= 3 ? 6 : tau <= 5 ? 4 : 0;
#pragma unroll
        for (int ww = 7; ww >= 0; ww--) {
            const int n = 7 - ww;
            acc[tau] = mfma16(kn[n], bc[ww], acc[tau]);
            SCHED();
            if (n >= 3 && n - 3 < nb) c[n - 3].x = pk(d[n - 3][0], d[n - 3][1]);
            if (n >= 4) { u4[n - 4] = (n & 1) ? bfhi(uu[(n - 4) >> 1]) : bflo(uu[(n - 4) >> 1]); PIN(u4[n - 4]); }
            SCHED();
            if (n < nb) d[n] = mfma16(bc[(n == 2 || n == 3 || n == 5) ? 8 : 7], mk[n < 4 ? (n & 1) : n - 2], zero4);
            if (n == 6 && tau + 2 < 8) acc[tau + 2] = mfma16(kn[16], bc[7], acc[tau + 2]);
            if (n == 7 && tau + 4 < 8) acc[tau + 4] = mfma16(ks, bc[7], acc[tau + 4]);
            SCHED();
            if (n >= 3 && n - 3 < nb) c[n - 3].y = pk(d[n - 3][2], d[n - 3][3]);
            SCHED();
        }
        PAD(101 + 3 * tau);
        if (tau == 3) {
            waitf(lds + FL_OFF + (F_UG + 4 + (lane & 3)) * 4, 1);
            if (tid == 0) __builtin_amdgcn_raw_buffer_store_b32(tag, rs, OFF_FL + pb * 4, 0, SC1);
        }
#pragma unroll
        for (int ww = 7; ww >= 0; ww--) {
            const int n = 7 - ww;
            if (tau < 7) {
                acc[tau + 1] = mfma16(kn[15 - ww], bc[ww], acc[tau + 1]);
                SCHED();
                bc[ww] = lld<v4u>(ub + (8 * tau + 1 + ww) * 64);
                if (ww == 7 && tau < 5) bc[8] = lld<v4u>(ub + (8 * tau + 8) * 64 - 16);
            }
            if (n == 0 && nb == 6) c[5] = CV(d[5]);
            if (n == 1 || n == 2) { for (int e = 0; e < 2; e++) { o[2 * n - 2 + e] = __builtin_fmaf(u4[2 * n - 2 + e], dm1, acc[tau][2 * n - 2 + e]); PIN(o[2 * n - 2 + e]); } }
            if (n == 3 || n == 4) { for (int e = 0; e < 2; e++) { o[2 * n - 6 + e] = __builtin_fmaf(gg[2 * n - 6 + e], o[2 * n - 6 + e], u4[2 * n - 6 + e]); PIN(o[2 * n - 6 + e]); } }
            if (n == 5) lst<v2u>(sb + tau * 512, (v2u){pk(o[0], o[1]), pk(o[2], o[3])});
            if (n == 6) { CFENCE(); lst<u32>(pg, (u32)(tau + 1)); }
            SCHED();
        }
        PAD(102 + 3 * tau);
        if (tau <= 5) {
            const v4u a0 = {c[0].x, c[0].y, c[1].x, c[1].y}, a1 = {c[2].x, c[2].y, c[3].x, c[3].y};
#pragma unroll
            for (int s = 0; s < 3; s++) if (tau + 2 + s < 8) acc[tau + 2 + s] = mfma16(a0, k2[s][0], acc[tau + 2 + s]);
#pragma unroll
            for (int s = 0; s < 3; s++) if (tau + 2 + s < 8) acc[tau + 2 + s] = mfma16(a1, k2[s][1], acc[tau + 2 + s]);
        }
        if (tau <= 3) {
            const v4u a3 = {c[4].x, c[4].y, c[5].x, c[5].y};
#pragma unroll
            for (int s = 0; s < 4; s++)
                if (tau + 4 + s < 8) acc[tau + 4 + s] = mfma16(a3, k3[s], acc[tau + 4 + s]);
        }
        SCHED();
        if (HELP == 2 && tau == 1 && wave < 4) l2pull(hdn + 4 * 14 * 2048, lds0 + DUMP_OFF);
    }
    STAMP(9); STAMPC(13);
    PAD(140);
    if (wave >= 4) { STAMP(11); STAMP_OUT(); return; }
    if (wave != 0) {
        const lptr ob = lds + S_OFF + (i >> 2) * WR + 32 * g, pr = lds + FL_OFF + (F_PROG + (lane & 7)) * 4;
#pragma unroll 1
        for (int c = wave - 1; c < 32; c += 3) {
            waitf(pr, (u32)(c >> 2) + 1u);
            const lptr o = ob + 128 * c + 8 * ((i & 3) ^ (c & 3));
            const v2u a0 = ldtr(o), a1 = ldtr(o + 4 * WR);
            *(v4u*)((char*)out + (u32)((64 * c + lane) * 2048 + 16 * pb)) = (v4u){a0.x, a0.y, a1.x, a1.y};
        }
        STAMP(5);
    } else {
        const int ho = OFF_H3 + pb * 7168 + lane * 16;
        v4u q[7];
        while ((u32)__builtin_amdgcn_s_memrealtime() - tstart < (u32)HTIME) __builtin_amdgcn_s_sleep(2);
        fetch<7, 0, 1024, true, 141>(q, nil, rs, ho, 0, tag);
        STAMP(10);
        PAD(143);
        f32x4 y = {0, 0, 0, 0};
#pragma unroll
        for (int k = 0; k < 7; k++) y += (f32x4){bflo(q[k].y), bfhi(q[k].y), bflo(q[k].w), bfhi(q[k].w)};
#pragma unroll
        for (int e = 0; e < 4; e++) {
            float v = y[e];
            const auto s16 = __builtin_amdgcn_permlane16_swap(__float_as_int(v), __float_as_int(v), false, false);
            v = __int_as_float(s16[0]) + __int_as_float(s16[1]);
            const auto s32 = __builtin_amdgcn_permlane32_swap(__float_as_int(v), __float_as_int(v), false, false);
            y[e] = __int_as_float(s32[0]) + __int_as_float(s32[1]);
        }
        if (lane < 16) { lst<f32x4>(lds + FIX_OFF + lane * 16, y); lst<u32>(lds + FIX_OFF + 256 + (lane & 7) * 4, q[0].z ^ tag); }
        SETF(F_FIX, 1);
        STAMP_OUT();
        return;
    }
    PAD(145);
    WAITF(F_FIX, 1);
    {
        const int tok = lane >> 3, e = lane & 7;
        const u32 row = lld<u32>(lds + FIX_OFF + 256 + tok * 4) & 2047u;
        const float base = __uint_as_float((u32)lld<u16>(lds + S_OFF + e * WR + ((row * 2) ^ ((row >> 3) & 24))) << 16);
        const float o = base + (1.f - lld<float>(lds + G_OFF + row * 4)) * lld<float>(lds + FIX_OFF + (e >> 2) * 128 + tok * 16 + (e & 3) * 4);
        if ((int)((row >> 6) % 3u) == wave - 1) out[row * 1024 + 8 * pb + e] = (u16)pk(o, o);
    }
    STAMP(11);
    STAMP_OUT();
}

extern "C" void kernel_launch(void* const* d_in, const int* in_sizes, int n_in, void* d_out, int out_size, void* d_ws,
                              size_t ws_size, hipStream_t stream) {
    fused_kernel<<<256, 512, 0, stream>>>((const u16*)d_in[0], (const u16*)d_in[8], (const u16*)d_in[12], (const u16*)d_in[5],
                                          (const u16*)d_in[1], (const u16*)d_in[2], (const u16*)d_in[3], (const u16*)d_in[4],
                                          (char*)d_ws + WS_BASE, (const u16*)d_in[6], (const u16*)d_in[9], (const u16*)d_in[10],
                                          (const u16*)d_in[11], (u16*)d_out);
}
```

```cpp
#include <hip/hip_runtime.h>
#include <stdint.h>

typedef unsigned short u16;
typedef unsigned u32;
typedef unsigned long long u64;
typedef u32 v4u __attribute__((ext_vector_type(4)));
typedef u32 v2u __attribute__((ext_vector_type(2)));
typedef float f32x2 __attribute__((ext_vector_type(2)));
typedef float f32x4 __attribute__((ext_vector_type(4)));
typedef float f32x16 __attribute__((ext_vector_type(16)));
typedef short bf16x8 __attribute__((ext_vector_type(8)));
typedef short s16x4 __attribute__((ext_vector_type(4)));
typedef __bf16 bf2_t __attribute__((ext_vector_type(2)));
#define LAS __attribute__((address_space(3)))
typedef LAS char* lptr;
#define GAS __attribute__((address_space(1)))
typedef GAS char* gptr;
typedef u64 v8q __attribute__((ext_vector_type(8)));

#define L_ 2048
#define D_ 1024
#define F_ 3584

#define OFF_E 0
#define OFF_F 0x1000
#define OFF_F2 0x1800
#define OFF_G 0x2000
#define OFF_P 0x10000
#define OFF_IN 0x50000
#define OFF_H3 OFF_IN
#define OFF_H2 (OFF_IN + 0x70000)
#define OFF_KT (OFF_IN + 0xe0000)
#define KTP 3072
#define WS_USED (OFF_KT + 512 * KTP)
#define OFF_STAMP 0x1000000
#define SC1 16
#define NT 2
#define SPIN_CAP 20000
#define LSPIN_CAP 400000

#define FL_OFF 0
#define PR_OFF 256
#define SKIP_OFF 512
#define DUMP_OFF 1024
#define ROWB 528
#define KROWS 11648
#define F_TAGV 28
#define F_TAG 29
#define F_PUB 30
#define W_OFF 1536
#define S_OFF (W_OFF + 4 * KROWS)
#define U_OFF (S_OFF + 512)
#define UP 4672
#define T_OFF (S_OFF + 8 * UP)
#define T_P2 2304
#define T_P3 3584
#define TP 4352
#define G_OFF (T_OFF + 8 * TP)
#define MSK_OFF (G_OFF + 8192)
#define F_UG 8
#define F_GT 16
#define F_SKIP 24
#define F_CLR 25
#define GU_OFF W_OFF
#define XS_OFF (GU_OFF + 131072 + 2 * KROWS)
#define HS_OFF (XS_OFF + 4096)
#define KEY_OFF (HS_OFF + 8 * 272)
#define LDS_BYTES (KEY_OFF + 256)
static_assert(LDS_BYTES >= MSK_OFF + 5120 && LDS_BYTES <= 163840, "");
#define F_XS 1
#define F_KG 16
#define F_GU 2
#define F_FR 4
#define F_H 8

#define STAMP(k) do { } while (0)
#define STAMPC(k) do { } while (0)
#define STAMP_OUT() do { } while (0)

#define WIN 12
#define PSLEEP 1
#define CODE_LINES 198
#define RTIME 300
#define NCLR 4
#define WS_BASE 0
#define PFIRST 1
#define PREW 1
#define RAMP 3
#define CWIN 6
#define KTIME 20
#define CTIME 320
#define GTIME 370
#define FTIME 470
#define XDELAY 18
#define NPULL 0
#define CPULL 0
#define PTIME 8
#define WDELAY 0
#define H2DELAY 4
#define DWIN 16
#define EWIN 4
#define DEARLY 8
#define DAT 30
#define SAMEX 0
#define HTIME 600
#define USHIFT 2
#define XLD(T, p) __builtin_nontemporal_load((const T*)(p))
#define BARRIER() asm volatile("s_waitcnt lgkmcnt(0)\n\ts_barrier" ::: "memory")
#define END() return
#define CFENCE() asm volatile("" ::: "memory")
#define SCHED() __builtin_amdgcn_sched_barrier(0)

__device__ __forceinline__ int lane_id() {
    int l;
    asm volatile("v_mbcnt_lo_u32_b32 %0, -1, 0\n\tv_mbcnt_hi_u32_b32 %0, -1, %0" : "=v"(l));
    __builtin_assume(l >= 0 && l < 64);
    return l;
}
__device__ __forceinline__ float bflo(u32 w) { return __uint_as_float(w << 16); }
__device__ __forceinline__ float bfhi(u32 w) { return __uint_as_float(w & 0xffff0000u); }
__device__ __forceinline__ u32 pk(float lo, float hi) {
    f32x2 v = {lo, hi};
    return __builtin_bit_cast(u32, __builtin_convertvector(v, bf2_t));
}
__device__ __forceinline__ float rbf(float f) { return bflo(pk(f, f)); }
__device__ __forceinline__ f32x4 mfma16(v4u a, v4u b, f32x4 c) {
    return __builtin_amdgcn_mfma_f32_16x16x32_bf16(__builtin_bit_cast(bf16x8, a), __builtin_bit_cast(bf16x8, b), c, 0, 0, 0);
}
__device__ __forceinline__ v2u ldtr(lptr p) {
    return __builtin_bit_cast(v2u, __builtin_amdgcn_ds_read_tr16_b64_v4i16((LAS s16x4*)p));
}
template <typename T> __device__ __forceinline__ T lld(lptr p) { return *(LAS T*)p; }
template <typename T> __device__ __forceinline__ void lst(lptr p, T v) { *(LAS T*)p = v; }
__device__ __forceinline__ float sigm(float s) { return __builtin_amdgcn_rcpf(1.f + __expf(-s)); }

__device__ __forceinline__ u32 umax(u32 a, u32 b) { return a > b ? a : b; }
__device__ __forceinline__ u32 umin(u32 a, u32 b) { return a < b ? a : b; }
__device__ __forceinline__ u32 wave_max(u32 v) {
    v = umax(v, (u32)__builtin_amdgcn_update_dpp(0, (int)v, 0x111, 0xf, 0xf, false));
    v = umax(v, (u32)__builtin_amdgcn_update_dpp(0, (int)v, 0x112, 0xf, 0xf, false));
    v = umax(v, (u32)__builtin_amdgcn_update_dpp(0, (int)v, 0x114, 0xf, 0xf, false));
    v = umax(v, (u32)__builtin_amdgcn_update_dpp(0, (int)v, 0x118, 0xf, 0xf, false));
    v = umax(v, (u32)__builtin_amdgcn_update_dpp(0, (int)v, 0x142, 0xa, 0xf, false));
    v = umax(v, (u32)__builtin_amdgcn_update_dpp(0, (int)v, 0x143, 0xc, 0xf, false));
    return (u32)__builtin_amdgcn_readlane((int)v, 63);
}

#define TAGOK(q) (((q).x == tag) & ((q).z == tag))
#define WAITN(n) asm volatile("s_waitcnt vmcnt(%0)" :: "i"(n) : "memory")
#define WAITV(n) asm volatile("s_waitcnt vmcnt(" #n ")" ::: "memory")

#define BRKS 5,0,100,141,44,65
constexpr int is_brk(int n) { const int l[] = {BRKS}; for (int v : l) if (v == n) return 1; return 0; }
#define PAD_TXT(n, b) ".if %c" #b "\n\ts_branch GMX_PAD%c" #n "\n\t.p2align 12\n\t.endif\nGMX_PAD%c" #n ":\n\ts_cbranch_execz GMX_RET"
#define PAD(n) asm volatile(PAD_TXT(0, 1) :: "i"(n), "i"(is_brk(n)))
#define PADV(n, v) asm volatile(PAD_TXT(1, 2) : "+v"(v) : "i"(n), "i"(is_brk(n)))
#define COLD_BEGIN() asm volatile("GMX_COLDB:" ::: "memory")
#define COLD_END() asm volatile("GMX_COLDE:" ::: "memory")
#define NPT 32
#define NTC 6
#define PADTAB "GMX_PAD58,GMX_PAD69,GMX_PAD65,GMX_PAD66,GMX_PAD42,GMX_PAD43,GMX_PAD44,GMX_PAD87,GMX_PAD85,GMX_RET,GMX_RET,GMX_RET,GMX_RET,GMX_RET,GMX_RET,GMX_RET,GMX_PAD30,GMX_PAD100,GMX_PAD141,GMX_RET,GMX_RET,GMX_RET,GMX_RET,GMX_RET,GMX_RET,GMX_RET,GMX_RET,GMX_RET,GMX_RET,GMX_RET,GMX_RET,GMX_RET"
#define TOUCH_HEAD                                   \
    "s_mov_b64 s[84:85], exec\n\t"                   \
    "s_getpc_b64 s[80:81]\n"                         \
    "GMX_L%=:\n\t"                                   \
    "s_lshl_b32 s82, %0, 2\n\t"                      \
    "s_add_i32 s82, s82, GMX_TAB-GMX_L%=\n\t"        \
    "s_ashr_i32 s83, s82, 31\n\t"                    \
    "s_add_u32 s80, s80, s82\n\t"                    \
    "s_addc_u32 s81, s81, s83\n\t"                   \
    "s_mov_b64 exec, 0\n\t"                          \
    "s_swappc_b64 s[82:83], s[80:81]\n\t"            \
    "s_mov_b64 exec, s[84:85]\n\t"
#define TOUCH_CLOB "s80", "s81", "s82", "s83", "s84", "s85", "scc"
#define TOUCH(idx) asm volatile(TOUCH_HEAD :: "s"(idx) : TOUCH_CLOB)
#define TOUCH_TAB(idx) asm volatile(TOUCH_HEAD "s_branch GMX_END\nGMX_TAB:\n\t.irp t," PADTAB "\n\ts_branch \\t\n\t.endr\nGMX_END:" :: "s"(idx) : TOUCH_CLOB)
#define EARLY0 "GMX_PAD5"
#define EARLY1 "GMX_PAD0"
#define TOUCH_EARLY(sel) asm volatile("s_getpc_b64 s[82:83]\nGMX_E0:\n\ts_add_u32 s82, s82, GMX_E1-GMX_E0\n\ts_addc_u32 s83, s83, 0\n\ts_mov_b64 exec, 0\n\t"      \
    "s_bitcmp1_b32 %0, 0\n\ts_cbranch_scc1 " EARLY1 "\n\ts_branch " EARLY0 "\nGMX_RET:\n\ts_setpc_b64 s[82:83]\nGMX_E1:\n\ts_mov_b64 exec, -1" :: "s"(sel) : TOUCH_CLOB)
template <int N, int STR>
__device__ __forceinline__ void request(v4u (&q)[N], __amdgpu_buffer_rsrc_t rs, int off) {
#pragma unroll
    for (int k = 0; k < N; k++) q[k] = __builtin_amdgcn_raw_buffer_load_b128(rs, off, k * STR, SC1);
}
__device__ __forceinline__ u32 umax3(u32 a, u32 b, u32 c) { u32 r; asm("v_max3_u32 %0, %1, %2, %3" : "=v"(r) : "v"(a), "v"(b), "v"(c)); return r; }
__device__ __forceinline__ u32 umed3(u32 a, u32 b, u32 c) { u32 r; asm("v_med3_u32 %0, %1, %2, %3" : "=v"(r) : "v"(a), "v"(b), "v"(c)); return r; }
template <int N, bool TAGGED>
__device__ __forceinline__ bool arrived(const v4u (&q)[N], u32 tag) {
    u32 aa = ~0u, oo = 0u;
#pragma unroll
    for (int k = 0; k < N; k++) {
        if (TAGGED) { aa &= q[k].x & q[k].z; oo |= q[k].x | q[k].z; }
        else oo = umax3(umax3(oo, q[k].x, q[k].y), q[k].z, q[k].w);
    }
    return TAGGED ? (aa == tag) & (oo == tag) : oo != ~0u;
}
template <int N, int STR, bool TAGGED, int PID>
__device__ __forceinline__ void fetch(v4u (&q)[N], __amdgpu_buffer_rsrc_t rs, int off, u32 tag) {
    bool ok = false;
#pragma unroll 1
    for (int spins = 0; spins < SPIN_CAP; ++spins) {
        PAD(PID);
        CFENCE();
        if (!ok) request<N, STR>(q, rs, off);
        ok = arrived<N, TAGGED>(q, tag);
        if (__all(ok)) break;
        if (PSLEEP) __builtin_amdgcn_s_sleep(PSLEEP);
    }
}
__device__ __forceinline__ u32 pkd(float lo, float hi) { return umin(pk(lo, hi), 0xfffffffeu); }
#define DPP(v, ctl) ((u32)__builtin_amdgcn_update_dpp(0, (int)(v), ctl, 0xf, 0xf, true))
template <bool FIRST>
__device__ __forceinline__ void top8(u32& a, u32& b, int lane) {
    u32 t;
    const u32 c1 = (lane & 1) ? 0u : ~0u, c2 = (lane & 2) ? 0u : ~0u;
#define FLIP(ctl, c) t = umed3(a, DPP(b, ctl), c); b = umed3(b, DPP(a, ctl), c); a = t
#define HALF(ctl, c) t = umed3(a, DPP(a, ctl), c); b = umed3(b, DPP(b, ctl), c); a = t
#define INTRA() t = umax(a, b); b = umin(a, b); a = t
#define RESORT() HALF(0x4e, c2); HALF(0xb1, c1); INTRA()
    if (FIRST) {
        INTRA();
        FLIP(0xb1, c1); INTRA();
        FLIP(0x1b, c2); HALF(0xb1, c1); INTRA();
    }
    t = umax(a, DPP(b, 0x141)); b = umax(b, DPP(a, 0x141)); a = t; RESORT();
    t = umax(a, DPP(b, 0x140)); b = umax(b, DPP(a, 0x140)); a = t; RESORT();
    {
        const auto sa = __builtin_amdgcn_permlane16_swap(a, a, false, false), sb = __builtin_amdgcn_permlane16_swap(b, b, false, false);
        a = umax(sa[0], DPP(sb[1], 0x1b)); b = umax(sb[0], DPP(sa[1], 0x1b)); RESORT();
    }
    if (FIRST) {
        const auto sa = __builtin_amdgcn_permlane32_swap(a, a, false, false), sb = __builtin_amdgcn_permlane32_swap(b, b, false, false);
        a = umax(sa[0], DPP(sb[1], 0x1b)); b = umax(sb[0], DPP(sa[1], 0x1b)); RESORT();
    }
}
template <int PID>
__device__ __forceinline__ u32 dots(__amdgpu_buffer_rsrc_t rs, int off, u32 tag) {
    v4u q[4];
    fetch<4, 1024, true, PID>(q, rs, off, tag);
    const f32x2 lo = ((f32x2){bflo(q[0].y), bfhi(q[0].y)} + (f32x2){bflo(q[1].y), bfhi(q[1].y)}) + ((f32x2){bflo(q[2].y), bfhi(q[2].y)} + (f32x2){bflo(q[3].y), bfhi(q[3].y)});
    const f32x2 hi = ((f32x2){bflo(q[0].w), bfhi(q[0].w)} + (f32x2){bflo(q[1].w), bfhi(q[1].w)}) + ((f32x2){bflo(q[2].w), bfhi(q[2].w)} + (f32x2){bflo(q[3].w), bfhi(q[3].w)});
    const auto s0 = __builtin_amdgcn_permlane32_swap(__float_as_uint(lo[0]), __float_as_uint(hi[0]), false, false), s1 = __builtin_amdgcn_permlane32_swap(__float_as_uint(lo[1]), __float_as_uint(hi[1]), false, false);
    return pk(__uint_as_float(s0[0]) + __uint_as_float(s0[1]), __uint_as_float(s1[0]) + __uint_as_float(s1[1]));
}
struct c2 { f32x2 r, i; };
__device__ __forceinline__ c2 cmul(c2 a, c2 b) { return c2{a.r * b.r - a.i * b.i, a.r * b.i + a.i * b.r}; }
__device__ __forceinline__ c2 csq(c2 a) { const f32x2 t = a.r * a.i; return c2{a.r * a.r - a.i * a.i, t + t}; }
__device__ __forceinline__ c2 cpow(f32x2 l2, f32x2 rev) {
    c2 z;
#pragma unroll
    for (int e = 0; e < 2; e++) {
        const float m = __builtin_amdgcn_exp2f(l2[e]);
        const float f = __builtin_amdgcn_fractf(rev[e]);
        z.r[e] = m * __builtin_amdgcn_cosf(f);
        z.i[e] = m * __builtin_amdgcn_sinf(f);
    }
    return z;
}
typedef _Float16 h2_t __attribute__((ext_vector_type(2)));
typedef _Float16 h8_t __attribute__((ext_vector_type(8)));
__device__ __forceinline__ u32 pkh(float lo, float hi) { f32x2 v = {lo, hi}; return __builtin_bit_cast(u32, __builtin_convertvector(v, h2_t)); }
__device__ __forceinline__ v4u cmulh4(v4u a, v4u b) {
    u32 r0, r1, r2, r3;
    asm("v_pk_mul_f16 %0, %4, %8 op_sel_hi:[0,1]\n\tv_pk_mul_f16 %1, %5, %9 op_sel_hi:[0,1]\n\t"
        "v_pk_mul_f16 %2, %6, %10 op_sel_hi:[0,1]\n\tv_pk_mul_f16 %3, %7, %11 op_sel_hi:[0,1]\n\t"
        "v_pk_fma_f16 %0, %4, %8, %0 op_sel:[1,1,0] op_sel_hi:[1,0,1] neg_lo:[0,1,0]\n\tv_pk_fma_f16 %1, %5, %9, %1 op_sel:[1,1,0] op_sel_hi:[1,0,1] neg_lo:[0,1,0]\n\t"
        "v_pk_fma_f16 %2, %6, %10, %2 op_sel:[1,1,0] op_sel_hi:[1,0,1] neg_lo:[0,1,0]\n\tv_pk_fma_f16 %3, %7, %11, %3 op_sel:[1,1,0] op_sel_hi:[1,0,1] neg_lo:[0,1,0]"
        : "=&v"(r0), "=&v"(r1), "=&v"(r2), "=&v"(r3) : "v"(a[0]), "v"(a[1]), "v"(a[2]), "v"(a[3]), "v"(b[0]), "v"(b[1]), "v"(b[2]), "v"(b[3]));
    return (v4u){r0, r1, r2, r3};
}
__device__ __forceinline__ v2u cpk(c2 a) { return (v2u){pkh(a.r[0], a.i[0]), pkh(a.r[1], a.i[1])}; }
__device__ __forceinline__ v4u group_mask(int m, int lane) {
    const u32 lo = (0x34563u >> (4 * m)) & 15u, w = (0x18642u >> (4 * m)) & 15u, one = m < 4 ? 0x3f80u : 0xffffu;
    const u32 x = (u32)((lane & 15) + 16 - 8 * (lane >> 4)) - lo;
    v4u r;
#pragma unroll
    for (int d = 0; d < 4; d++) r[d] = (x - 2 * d <= w ? one : 0u) | (x - 2 * d - 1 <= w ? one << 16 : 0u);
    return r;
}
__device__ __forceinline__ void dma16cs(gptr sbase, u32 voff, u32 lds_dst) {
    asm volatile("s_mov_b32 m0, %2\n\ts_nop 0\n\tglobal_load_lds_dwordx4 %0, %1" :: "v"(voff), "s"(sbase), "s"(lds_dst) : "memory");
}
__device__ __forceinline__ void dma4s(gptr sbase, u32 voff, u32 lds_dst, bool nt) {
    if (nt) asm volatile("s_mov_b32 m0, %2\n\ts_nop 0\n\tglobal_load_lds_dword %0, %1 nt" :: "v"(voff), "s"(sbase), "s"(lds_dst) : "memory");
    else asm volatile("s_mov_b32 m0, %2\n\ts_nop 0\n\tglobal_load_lds_dword %0, %1" :: "v"(voff), "s"(sbase), "s"(lds_dst) : "memory");
}
template <int CTL> __device__ __forceinline__ float addd(float a, float b) {
    float r = __uint_as_float(DPP(__float_as_uint(a), CTL)) + b;
    asm("" : "+v"(r));
    return r;
}
#define SETF(f, v) do { CFENCE(); lst<u32>(lds + FL_OFF + (f) * 4, (u32)(v)); CFENCE(); } while (0)
__device__ __forceinline__ void waitf(lptr a, u32 v) {
#pragma unroll 1
    for (int spins = 0; spins < LSPIN_CAP; ++spins) {
        CFENCE();
        if (__all(lld<u32>(a) >= v)) break;
        __builtin_amdgcn_s_sleep(1);
    }
    CFENCE();
}
#define WAITF(f, v) waitf(lds + FL_OFF + (f) * 4, v)
__device__ __forceinline__ void swatch(gptr p, u32 tag) {
    asm volatile("s_movk_i32 s80, 100\n"
                 "GMX_W%=:\n\ts_load_dword s81, %0, 0 glc\n\ts_waitcnt lgkmcnt(0)\n\ts_cmp_eq_u32 s81, %1\n\ts_cbranch_scc1 GMX_X%=\n\t"
                 "s_addk_i32 s80, -1\n\ts_cmp_lg_u32 s80, 0\n\ts_cbranch_scc1 GMX_W%=\n"
                 "GMX_X%=:" :: "s"(p), "s"(tag) : "memory", "scc", "s80", "s81");
}
#define MKRS() __builtin_amdgcn_make_buffer_rsrc((void*)ws, 0, WS_USED, 0x00020000)
#define MKTAG(e) ((((e) + 1u) * 0x9E3779B1u ^ 0x5bd1e995u) & ~1u)
#define EPOCH() (*(const __attribute__((address_space(4))) u32*)(u64)(ws + OFF_E))

#define PADL(n) ".if %c[b" #n "]\ns_branch GMX_PAD" #n "\n.p2align 12\nGMX_PAD" #n ":\ns_cbranch_execz GMX_RET\n.elseif %c[all]\nGMX_PAD" #n ":\ns_cbranch_execz GMX_RET\n.else\ns_nop 0\n.endif\n"
#define LOOP_BRK(n) , [b##n] "i"(is_brk(n))
#define LOOP_BRKS , [all] "i"(is_brk(-1)) LOOP_BRK(100) LOOP_BRK(101) LOOP_BRK(102) LOOP_BRK(103) LOOP_BRK(104) LOOP_BRK(105) LOOP_BRK(106)
#define CLOB10(a) "v" #a "0", "v" #a "1", "v" #a "2", "v" #a "3", "v" #a "4", "v" #a "5", "v" #a "6", "v" #a "7", "v" #a "8", "v" #a "9"
#define LOOP_CLOB CLOB10(), CLOB10(1), CLOB10(2), CLOB10(3), CLOB10(4), CLOB10(5), CLOB10(6), CLOB10(7), CLOB10(8), CLOB10(9), CLOB10(10), CLOB10(11), \
    CLOB10(12), CLOB10(13), CLOB10(14), CLOB10(15), CLOB10(16), CLOB10(17), CLOB10(18), CLOB10(19), CLOB10(20), CLOB10(21), CLOB10(22), CLOB10(23), \
    "vcc", "scc", "s80", "memory"
#define CONV_LOOP \
    "ds_read_b128 v[188:191], %[ub] offset:448\n" "ds_read_b128 v[112:115], %[ma] offset:0\n" "ds_read_b128 v[116:119], %[ma] offset:1024\n" \
    "ds_read_b128 v[192:195], %[ub] offset:432\n" "ds_read_b128 v[120:123], %[ma] offset:2048\n" "ds_read_b128 v[124:127], %[ma] offset:3072\n" \
    "v_add_u32 v156, 0x400, %[pn]\n" "ds_read_b128 v[160:163], %[ub] offset:0\n" "ds_read2_b32 v[28:29], v156 offset0:32 offset1:34\n" \
    "ds_read2_b32 v[30:31], v156 offset0:36 offset1:38\n" "ds_read2_b32 v[60:61], %[pn] offset0:32 offset1:34\n" \
    "ds_read2_b32 v[62:63], %[pn] offset0:36 offset1:38\n" "ds_read_b128 v[164:167], %[ub] offset:64\n" \
    "ds_read2_b32 v[24:25], v156 offset0:64 offset1:66\n" "ds_read2_b32 v[26:27], v156 offset0:68 offset1:70\n" "s_waitcnt lgkmcnt(12)\n" \
    "v_mfma_f32_16x16x32_bf16 v[196:199], v[188:191], v[112:115], 0\n" "ds_read2_b32 v[56:57], %[pn] offset0:64 offset1:66\n" \
    "ds_read2_b32 v[58:59], %[pn] offset0:68 offset1:70\n" "s_waitcnt lgkmcnt(13)\n" "v_mfma_f32_16x16x32_bf16 v[200:203], v[188:191], v[116:119], 0\n" \
    "ds_read_b128 v[168:171], %[ub] offset:128\n" "ds_read2_b32 v[20:21], v156 offset0:96 offset1:98\n" \
    "ds_read2_b32 v[22:23], v156 offset0:100 offset1:102\n" "s_waitcnt lgkmcnt(15)\n" \
    "v_mfma_f32_16x16x32_bf16 v[204:207], v[192:195], v[112:115], 0\n" "ds_read2_b32 v[52:53], %[pn] offset0:96 offset1:98\n" \
    "ds_read2_b32 v[54:55], %[pn] offset0:100 offset1:102\n" "v_mfma_f32_16x16x32_bf16 v[208:211], v[192:195], v[116:119], 0\n" \
    "ds_read_b128 v[172:175], %[ub] offset:192\n" "ds_read2_b32 v[16:17], v156 offset0:128 offset1:130\n" \
    "ds_read2_b32 v[18:19], v156 offset0:132 offset1:134\n" "v_cvt_pk_bf16_f32 v212, v196, v197\n" "v_cvt_pk_bf16_f32 v213, v198, v199\n" \
    "s_waitcnt lgkmcnt(15)\n" "v_mfma_f32_16x16x32_bf16 v[196:199], v[188:191], v[120:123], 0\n" \
    "ds_read2_b32 v[48:49], %[pn] offset0:128 offset1:130\n" "ds_read2_b32 v[50:51], %[pn] offset0:132 offset1:134\n" \
    "v_cvt_pk_bf16_f32 v214, v200, v201\n" "v_cvt_pk_bf16_f32 v215, v202, v203\n" "v_mfma_f32_16x16x32_bf16 v[200:203], v[192:195], v[124:127], 0\n" \
    "ds_read_b128 v[176:179], %[ub] offset:256\n" "ds_read2_b32 v[12:13], v156 offset0:160 offset1:162\n" \
    "ds_read2_b32 v[14:15], v156 offset0:164 offset1:166\n" "v_mfma_f32_16x16x32_bf16 v[128:131], v[28:31], v[160:163], 0\n" \
    "ds_read2_b32 v[64:65], %[pn] offset0:0 offset1:2\n" "ds_read2_b32 v[66:67], %[pn] offset0:4 offset1:6\n" "s_waitcnt lgkmcnt(15)\n" \
    "v_mfma_f32_16x16x32_bf16 v[132:135], v[60:63], v[160:163], 0\n" "ds_read2_b32 v[44:45], %[pn] offset0:160 offset1:162\n" \
    "ds_read2_b32 v[46:47], %[pn] offset0:164 offset1:166\n" "v_mfma_f32_16x16x32_bf16 v[128:131], v[24:27], v[164:167], v[128:131]\n" \
    "ds_read_b128 v[180:183], %[ub] offset:320\n" "ds_read2_b32 v[8:9], v156 offset0:192 offset1:194\n" \
    "ds_read2_b32 v[10:11], v156 offset0:196 offset1:198\n" "v_mfma_f32_16x16x32_bf16 v[132:135], v[56:59], v[164:167], v[132:135]\n" \
    "ds_read2_b32 v[40:41], %[pn] offset0:192 offset1:194\n" "ds_read2_b32 v[42:43], %[pn] offset0:196 offset1:198\n" "s_waitcnt lgkmcnt(15)\n" \
    "v_mfma_f32_16x16x32_bf16 v[128:131], v[20:23], v[168:171], v[128:131]\n" "ds_read_b128 v[184:187], %[ub] offset:384\n" \
    "ds_read2_b32 v[4:5], v156 offset0:224 offset1:226\n" "ds_read2_b32 v[6:7], v156 offset0:228 offset1:230\n" \
    "v_mfma_f32_16x16x32_bf16 v[132:135], v[52:55], v[168:171], v[132:135]\n" "ds_read2_b32 v[36:37], %[pn] offset0:224 offset1:226\n" \
    "ds_read2_b32 v[38:39], %[pn] offset0:228 offset1:230\n" "v_add_u32 v157, 0x800, %[pn]\n" "s_waitcnt lgkmcnt(15)\n" \
    "v_mfma_f32_16x16x32_bf16 v[128:131], v[16:19], v[172:175], v[128:131]\n" "ds_read2_b32 v[0:1], v157 offset0:0 offset1:2\n" \
    "ds_read2_b32 v[2:3], v157 offset0:4 offset1:6\n" "v_mov_b32 v235, %[k1]\n" "ds_read_u16 v235, v235\n" \
    "v_mfma_f32_16x16x32_bf16 v[132:135], v[48:51], v[172:175], v[132:135]\n" "ds_read2_b32 v[32:33], v156 offset0:0 offset1:2\n" \
    "ds_read2_b32 v[34:35], v156 offset0:4 offset1:6\n" "ds_read_b128 v[108:111], %[ma] offset:4096\n" "s_waitcnt lgkmcnt(15)\n" \
    "v_mfma_f32_16x16x32_bf16 v[128:131], v[12:15], v[176:179], v[128:131]\n" "v_add_u32 v158, 0x280, %[p2]\n" "v_add_u32 v159, 0x500, %[p2]\n" \
    "v_mfma_f32_16x16x32_bf16 v[136:139], v[64:67], v[188:191], 0\n" "ds_read_b128 v[160:163], %[ub] offset:512\n" \
    "v_cvt_pk_bf16_f32 v216, v204, v205\n" "v_mfma_f32_16x16x32_bf16 v[132:135], v[44:47], v[176:179], v[132:135]\n" \
    "v_cvt_pk_bf16_f32 v217, v206, v207\n" "v_cvt_pk_bf16_f32 v218, v208, v209\n" "s_waitcnt lgkmcnt(14)\n" \
    "v_mfma_f32_16x16x32_bf16 v[128:131], v[8:11], v[180:183], v[128:131]\n" "ds_read_b128 v[164:167], %[ub] offset:576\n" \
    "v_cvt_pk_bf16_f32 v219, v210, v211\n" "s_waitcnt lgkmcnt(13)\n" "v_mfma_f32_16x16x32_bf16 v[132:135], v[40:43], v[180:183], v[132:135]\n" \
    "v_cvt_pk_bf16_f32 v220, v196, v197\n" "v_cvt_pk_bf16_f32 v221, v198, v199\n" "s_waitcnt lgkmcnt(10)\n" \
    "v_mfma_f32_16x16x32_bf16 v[128:131], v[4:7], v[184:187], v[128:131]\n" "ds_read_b32 v234, %[pr] offset:64\n" \
    "ds_read_b128 v[168:171], %[ub] offset:640\n" "s_waitcnt lgkmcnt(10)\n" "v_mfma_f32_16x16x32_bf16 v[132:135], v[36:39], v[184:187], v[132:135]\n" \
    "ds_read2_b32 v[68:69], %[p2] offset0:32 offset1:34\n" "ds_read2_b32 v[70:71], %[p2] offset0:112 offset1:114\n" "s_waitcnt lgkmcnt(10)\n" \
    "v_mfma_f32_16x16x32_bf16 v[128:131], v[0:3], v[188:191], v[128:131]\n" "s_waitcnt lgkmcnt(9)\n" "v_mul_u32_u24 v235, 0x10001, v235\n" \
    "s_waitcnt lgkmcnt(6)\n" "v_and_b32 v108, v108, v235\n" "v_mfma_f32_16x16x32_bf16 v[132:135], v[32:35], v[188:191], v[132:135]\n" \
    "v_and_b32 v109, v109, v235\n" "v_and_b32 v110, v110, v235\n" "v_and_b32 v111, v111, v235\n" "ds_read2_b32 v[72:73], %[p2] offset0:16 offset1:18\n" \
    "ds_read2_b32 v[74:75], %[p2] offset0:96 offset1:98\n" "v_mfma_f32_16x16x32_bf16 v[144:147], v[108:111], v[188:191], 0\n" \
    "ds_read2_b32 v[76:77], %[p2] offset0:0 offset1:2\n" "ds_read2_b32 v[78:79], %[p2] offset0:80 offset1:82\n" PADL(100) "s_waitcnt lgkmcnt(9)\n" \
    "v_mfma_f32_16x16x32_bf16 v[132:135], v[28:31], v[160:163], v[132:135]\n" "ds_read_b128 v[172:175], %[ub] offset:704\n" \
    "v_cvt_pk_bf16_f32 v222, v200, v201\n" "v_mfma_f32_16x16x32_bf16 v[136:139], v[60:63], v[160:163], v[136:139]\n" \
    "ds_read2_b32 v[80:81], v158 offset0:32 offset1:34\n" "ds_read2_b32 v[82:83], v158 offset0:112 offset1:114\n" "s_waitcnt lgkmcnt(11)\n" \
    "v_mfma_f32_16x16x32_bf16 v[132:135], v[24:27], v[164:167], v[132:135]\n" "v_cvt_pk_bf16_f32 v223, v202, v203\n" \
    "v_mfma_f32_16x16x32_bf16 v[136:139], v[56:59], v[164:167], v[136:139]\n" "ds_read2_b32 v[84:85], v158 offset0:16 offset1:18\n" \
    "ds_read2_b32 v[86:87], v158 offset0:96 offset1:98\n" "s_waitcnt lgkmcnt(11)\n" \
    "v_mfma_f32_16x16x32_bf16 v[132:135], v[20:23], v[168:171], v[132:135]\n" "v_cmp_gt_u32 vcc, 1, v234\n" "s_cbranch_vccnz GMX_Sg\n" "GMX_Rg:\n" \
    "ds_read2_b32 v[88:89], v158 offset0:0 offset1:2\n" "ds_read2_b32 v[90:91], v158 offset0:80 offset1:82\n" "s_waitcnt lgkmcnt(11)\n" \
    "v_mfma_f32_16x16x32_bf16 v[136:139], v[212:215], v[68:71], v[136:139]\n" "ds_read_b128 v[176:179], %[ub] offset:768\n" \
    "v_mfma_f32_16x16x32_bf16 v[136:139], v[52:55], v[168:171], v[136:139]\n" "ds_read2_b32 v[92:93], v159 offset0:48 offset1:50\n" \
    "ds_read2_b32 v[94:95], v159 offset0:144 offset1:146\n" "s_waitcnt lgkmcnt(12)\n" "v_mfma_f32_16x16x32_bf16 v[140:143], v[212:215], v[72:75], 0\n" \
    "s_waitcnt lgkmcnt(10)\n" "v_mfma_f32_16x16x32_bf16 v[144:147], v[212:215], v[76:79], v[144:147]\n" \
    "ds_read2_b32 v[96:97], v159 offset0:32 offset1:34\n" "ds_read2_b32 v[98:99], v159 offset0:128 offset1:130\n" "s_waitcnt lgkmcnt(11)\n" \
    "v_mfma_f32_16x16x32_bf16 v[132:135], v[16:19], v[172:175], v[132:135]\n" "ds_read2_b32 v[100:101], v159 offset0:16 offset1:18\n" \
    "ds_read2_b32 v[102:103], v159 offset0:112 offset1:114\n" "s_waitcnt lgkmcnt(11)\n" \
    "v_mfma_f32_16x16x32_bf16 v[136:139], v[216:219], v[80:83], v[136:139]\n" "ds_read_b128 v[180:183], %[ub] offset:832\n" \
    "v_mfma_f32_16x16x32_bf16 v[136:139], v[48:51], v[172:175], v[136:139]\n" "ds_read2_b32 v[104:105], v159 offset0:0 offset1:2\n" \
    "ds_read2_b32 v[106:107], v159 offset0:96 offset1:98\n" "s_waitcnt lgkmcnt(12)\n" \
    "v_mfma_f32_16x16x32_bf16 v[140:143], v[216:219], v[84:87], v[140:143]\n" "s_waitcnt lgkmcnt(10)\n" \
    "v_mfma_f32_16x16x32_bf16 v[144:147], v[216:219], v[88:91], v[144:147]\n" "ds_read_b128 v[188:191], %[ub] offset:960\n" "s_waitcnt lgkmcnt(10)\n" \
    "v_mfma_f32_16x16x32_bf16 v[132:135], v[12:15], v[176:179], v[132:135]\n" "s_waitcnt lgkmcnt(8)\n" \
    "v_mfma_f32_16x16x32_bf16 v[144:147], v[220:223], v[92:95], v[144:147]\n" "ds_read_b128 v[184:187], %[ub] offset:896\n" \
    "v_mfma_f32_16x16x32_bf16 v[136:139], v[44:47], v[176:179], v[136:139]\n" "ds_read_b128 v[192:195], %[ub] offset:944\n" "s_waitcnt lgkmcnt(8)\n" \
    "v_mfma_f32_16x16x32_bf16 v[148:151], v[220:223], v[96:99], 0\n" "s_waitcnt lgkmcnt(6)\n" \
    "v_mfma_f32_16x16x32_bf16 v[152:155], v[220:223], v[100:103], 0\n" "ds_read_b64 v[224:225], %[eb] offset:0\n" "s_waitcnt lgkmcnt(6)\n" \
    "v_mfma_f32_16x16x32_bf16 v[132:135], v[8:11], v[180:183], v[132:135]\n" "ds_read_b128 v[226:229], %[gb] offset:0\n" "s_waitcnt lgkmcnt(5)\n" \
    "v_mfma_f32_16x16x32_bf16 v[156:159], v[220:223], v[104:107], 0\n" "ds_read_b128 v[160:163], %[ub] offset:1024\n" \
    "v_mfma_f32_16x16x32_bf16 v[136:139], v[40:43], v[180:183], v[136:139]\n" "ds_read_b128 v[164:167], %[ub] offset:1088\n" "s_waitcnt lgkmcnt(6)\n" \
    "v_mfma_f32_16x16x32_bf16 v[204:207], v[188:191], v[112:115], 0\n" "ds_read_b128 v[168:171], %[ub] offset:1152\n" \
    "v_mfma_f32_16x16x32_bf16 v[208:211], v[188:191], v[116:119], 0\n" "ds_read_b128 v[172:175], %[ub] offset:1216\n" "s_waitcnt lgkmcnt(7)\n" \
    "v_mfma_f32_16x16x32_bf16 v[132:135], v[4:7], v[184:187], v[132:135]\n" "ds_read_b128 v[176:179], %[ub] offset:1280\n" "s_waitcnt lgkmcnt(7)\n" \
    "v_mfma_f32_16x16x32_bf16 v[196:199], v[192:195], v[112:115], 0\n" "v_cvt_pk_bf16_f32 v212, v204, v205\n" "v_cvt_pk_bf16_f32 v213, v206, v207\n" \
    "v_mfma_f32_16x16x32_bf16 v[136:139], v[36:39], v[184:187], v[136:139]\n" "v_cvt_pk_bf16_f32 v214, v208, v209\n" \
    "v_cvt_pk_bf16_f32 v215, v210, v211\n" "v_mfma_f32_16x16x32_bf16 v[200:203], v[192:195], v[116:119], 0\n" "s_waitcnt lgkmcnt(6)\n" \
    "v_lshlrev_b32 v230, 16, v224\n" "v_and_b32 v231, 0xffff0000, v224\n" "v_mfma_f32_16x16x32_bf16 v[204:207], v[188:191], v[120:123], 0\n" \
    "v_lshlrev_b32 v232, 16, v225\n" "v_and_b32 v233, 0xffff0000, v225\n" "v_mfma_f32_16x16x32_bf16 v[132:135], v[0:3], v[188:191], v[132:135]\n" \
    "v_fmac_f32 v128, %[dm], v230\n" "v_fmac_f32 v129, %[dm], v231\n" "v_mfma_f32_16x16x32_bf16 v[208:211], v[192:195], v[124:127], 0\n" \
    "v_fmac_f32 v130, %[dm], v232\n" "v_fmac_f32 v131, %[dm], v233\n" "v_mfma_f32_16x16x32_bf16 v[136:139], v[32:35], v[188:191], v[136:139]\n" \
    "s_waitcnt lgkmcnt(2)\n" "v_fmac_f32 v230, v226, v128\n" "v_fmac_f32 v231, v227, v129\n" \
    "v_mfma_f32_16x16x32_bf16 v[140:143], v[64:67], v[188:191], v[140:143]\n" "v_fmac_f32 v232, v228, v130\n" "v_fmac_f32 v233, v229, v131\n" \
    "v_mfma_f32_16x16x32_bf16 v[148:151], v[108:111], v[188:191], v[148:151]\n" PADL(101) \
    "v_mfma_f32_16x16x32_bf16 v[136:139], v[28:31], v[160:163], v[136:139]\n" "v_cvt_pk_bf16_f32 v128, v230, v231\n" \
    "v_cvt_pk_bf16_f32 v129, v232, v233\n" "v_mfma_f32_16x16x32_bf16 v[140:143], v[60:63], v[160:163], v[140:143]\n" \
    "ds_write_b64 %[sb], v[128:129] offset:0\n" "v_cvt_pk_bf16_f32 v216, v196, v197\n" \
    "v_mfma_f32_16x16x32_bf16 v[140:143], v[212:215], v[68:71], v[140:143]\n" "v_cvt_pk_bf16_f32 v217, v198, v199\n" \
    "v_cvt_pk_bf16_f32 v218, v200, v201\n" "v_mfma_f32_16x16x32_bf16 v[136:139], v[24:27], v[164:167], v[136:139]\n" \
    "ds_write_b32 %[pg], %[pg] offset:0\n" "v_cvt_pk_bf16_f32 v219, v202, v203\n" \
    "v_mfma_f32_16x16x32_bf16 v[144:147], v[212:215], v[72:75], v[144:147]\n" "ds_read_b64 v[224:225], %[eb] offset:512\n" \
    "v_cvt_pk_bf16_f32 v220, v204, v205\n" "v_mfma_f32_16x16x32_bf16 v[140:143], v[56:59], v[164:167], v[140:143]\n" \
    "ds_read_b128 v[226:229], %[gb] offset:1024\n" "v_cvt_pk_bf16_f32 v221, v206, v207\n" \
    "v_mfma_f32_16x16x32_bf16 v[148:151], v[212:215], v[76:79], v[148:151]\n" "ds_read_b128 v[188:191], %[ub] offset:1472\n" \
    "v_cvt_pk_bf16_f32 v222, v208, v209\n" "v_mfma_f32_16x16x32_bf16 v[136:139], v[20:23], v[168:171], v[136:139]\n" \
    "ds_read_b128 v[180:183], %[ub] offset:1344\n" "v_cvt_pk_bf16_f32 v223, v210, v211\n" \
    "v_mfma_f32_16x16x32_bf16 v[140:143], v[216:219], v[80:83], v[140:143]\n" "ds_read_b128 v[192:195], %[ub] offset:1456\n" \
    "v_mfma_f32_16x16x32_bf16 v[140:143], v[52:55], v[168:171], v[140:143]\n" "ds_read_b128 v[184:187], %[ub] offset:1408\n" \
    "v_mfma_f32_16x16x32_bf16 v[144:147], v[216:219], v[84:87], v[144:147]\n" "ds_read_b128 v[160:163], %[ub] offset:1536\n" "s_waitcnt lgkmcnt(8)\n" \
    "v_mfma_f32_16x16x32_bf16 v[136:139], v[16:19], v[172:175], v[136:139]\n" "ds_read_b128 v[164:167], %[ub] offset:1600\n" \
    "v_mfma_f32_16x16x32_bf16 v[148:151], v[216:219], v[88:91], v[148:151]\n" "ds_read_b128 v[168:171], %[ub] offset:1664\n" \
    "v_mfma_f32_16x16x32_bf16 v[140:143], v[48:51], v[172:175], v[140:143]\n" "s_waitcnt lgkmcnt(8)\n" "v_lshlrev_b32 v230, 16, v224\n" \
    "v_and_b32 v231, 0xffff0000, v224\n" "v_mfma_f32_16x16x32_bf16 v[148:151], v[220:223], v[92:95], v[148:151]\n" "v_lshlrev_b32 v232, 16, v225\n" \
    "v_and_b32 v233, 0xffff0000, v225\n" "v_mfma_f32_16x16x32_bf16 v[136:139], v[12:15], v[176:179], v[136:139]\n" "v_fmac_f32 v132, %[dm], v230\n" \
    "v_fmac_f32 v133, %[dm], v231\n" "v_mfma_f32_16x16x32_bf16 v[152:155], v[220:223], v[96:99], v[152:155]\n" "v_fmac_f32 v134, %[dm], v232\n" \
    "v_fmac_f32 v135, %[dm], v233\n" "v_mfma_f32_16x16x32_bf16 v[156:159], v[220:223], v[100:103], v[156:159]\n" "s_waitcnt lgkmcnt(4)\n" \
    "v_fmac_f32 v230, v226, v132\n" "v_fmac_f32 v231, v227, v133\n" "v_mfma_f32_16x16x32_bf16 v[140:143], v[44:47], v[176:179], v[140:143]\n" \
    "v_fmac_f32 v232, v228, v134\n" "v_fmac_f32 v233, v229, v135\n" "v_mfma_f32_16x16x32_bf16 v[196:199], v[188:191], v[112:115], 0\n" \
    "v_cvt_pk_bf16_f32 v132, v230, v231\n" "v_cvt_pk_bf16_f32 v133, v232, v233\n" \
    "v_mfma_f32_16x16x32_bf16 v[136:139], v[8:11], v[180:183], v[136:139]\n" "ds_write_b64 %[sb], v[132:133] offset:512\n" \
    "v_mfma_f32_16x16x32_bf16 v[200:203], v[188:191], v[116:119], 0\n" "v_mfma_f32_16x16x32_bf16 v[140:143], v[40:43], v[180:183], v[140:143]\n" \
    "ds_write_b32 %[pg], %[pg] offset:32\n" "v_mfma_f32_16x16x32_bf16 v[204:207], v[192:195], v[112:115], 0\n" "v_cvt_pk_bf16_f32 v212, v196, v197\n" \
    "v_cvt_pk_bf16_f32 v213, v198, v199\n" "s_waitcnt lgkmcnt(2)\n" "v_mfma_f32_16x16x32_bf16 v[136:139], v[4:7], v[184:187], v[136:139]\n" \
    "ds_read_b64 v[224:225], %[eb] offset:1024\n" "v_cvt_pk_bf16_f32 v214, v200, v201\n" \
    "v_mfma_f32_16x16x32_bf16 v[208:211], v[192:195], v[116:119], 0\n" "v_cvt_pk_bf16_f32 v215, v202, v203\n" \
    "ds_read_b128 v[226:229], %[gb] offset:2048\n" "v_mfma_f32_16x16x32_bf16 v[140:143], v[36:39], v[184:187], v[140:143]\n" \
    "ds_read_b128 v[172:175], %[ub] offset:1728\n" "v_cvt_pk_bf16_f32 v216, v204, v205\n" \
    "v_mfma_f32_16x16x32_bf16 v[196:199], v[188:191], v[120:123], 0\n" "ds_read_b128 v[176:179], %[ub] offset:1792\n" \
    "v_cvt_pk_bf16_f32 v217, v206, v207\n" "v_mfma_f32_16x16x32_bf16 v[136:139], v[0:3], v[188:191], v[136:139]\n" \
    "v_cvt_pk_bf16_f32 v218, v208, v209\n" "v_cvt_pk_bf16_f32 v219, v210, v211\n" "v_mfma_f32_16x16x32_bf16 v[200:203], v[192:195], v[124:127], 0\n" \
    "ds_read_b128 v[180:183], %[ub] offset:1856\n" "v_mfma_f32_16x16x32_bf16 v[140:143], v[32:35], v[188:191], v[140:143]\n" \
    "v_cvt_pk_bf16_f32 v220, v196, v197\n" "v_cvt_pk_bf16_f32 v221, v198, v199\n" \
    "v_mfma_f32_16x16x32_bf16 v[144:147], v[64:67], v[188:191], v[144:147]\n" "ds_read_b128 v[192:195], %[ub] offset:1968\n" \
    "v_mfma_f32_16x16x32_bf16 v[152:155], v[108:111], v[188:191], v[152:155]\n" PADL(102) \
    "v_mfma_f32_16x16x32_bf16 v[140:143], v[28:31], v[160:163], v[140:143]\n" "s_waitcnt lgkmcnt(5)\n" "v_lshlrev_b32 v230, 16, v224\n" \
    "v_and_b32 v231, 0xffff0000, v224\n" "v_mfma_f32_16x16x32_bf16 v[144:147], v[60:63], v[160:163], v[144:147]\n" "v_lshlrev_b32 v232, 16, v225\n" \
    "v_and_b32 v233, 0xffff0000, v225\n" "v_mfma_f32_16x16x32_bf16 v[144:147], v[212:215], v[68:71], v[144:147]\n" "v_fmac_f32 v136, %[dm], v230\n" \
    "v_fmac_f32 v137, %[dm], v231\n" "v_mfma_f32_16x16x32_bf16 v[140:143], v[24:27], v[164:167], v[140:143]\n" "v_fmac_f32 v138, %[dm], v232\n" \
    "v_fmac_f32 v139, %[dm], v233\n" "v_mfma_f32_16x16x32_bf16 v[148:151], v[212:215], v[72:75], v[148:151]\n" "s_waitcnt lgkmcnt(2)\n" \
    "v_fmac_f32 v230, v226, v136\n" "v_fmac_f32 v231, v227, v137\n" "v_mfma_f32_16x16x32_bf16 v[144:147], v[56:59], v[164:167], v[144:147]\n" \
    "v_fmac_f32 v232, v228, v138\n" "v_fmac_f32 v233, v229, v139\n" "v_mfma_f32_16x16x32_bf16 v[152:155], v[212:215], v[76:79], v[152:155]\n" \
    "ds_read_b128 v[188:191], %[ub] offset:1984\n" "v_cvt_pk_bf16_f32 v136, v230, v231\n" \
    "v_mfma_f32_16x16x32_bf16 v[140:143], v[20:23], v[168:171], v[140:143]\n" "v_cvt_pk_bf16_f32 v137, v232, v233\n" \
    "ds_write_b64 %[sb], v[136:137] offset:1024\n" "v_mfma_f32_16x16x32_bf16 v[144:147], v[216:219], v[80:83], v[144:147]\n" \
    "v_cvt_pk_bf16_f32 v222, v200, v201\n" "v_cvt_pk_bf16_f32 v223, v202, v203\n" \
    "v_mfma_f32_16x16x32_bf16 v[144:147], v[52:55], v[168:171], v[144:147]\n" "ds_write_b32 %[pg], %[pg] offset:64\n" \
    "v_mfma_f32_16x16x32_bf16 v[148:151], v[216:219], v[84:87], v[148:151]\n" "ds_read_b32 v234, %[pr] offset:32\n" \
    "v_mfma_f32_16x16x32_bf16 v[140:143], v[16:19], v[172:175], v[140:143]\n" "ds_read_b128 v[184:187], %[ub] offset:1920\n" \
    "v_mfma_f32_16x16x32_bf16 v[152:155], v[216:219], v[88:91], v[152:155]\n" "ds_read_b64 v[224:225], %[eb] offset:1536\n" \
    "v_mfma_f32_16x16x32_bf16 v[144:147], v[48:51], v[172:175], v[144:147]\n" "ds_read_b128 v[226:229], %[gb] offset:3072\n" \
    "v_mfma_f32_16x16x32_bf16 v[152:155], v[220:223], v[92:95], v[152:155]\n" "v_mfma_f32_16x16x32_bf16 v[140:143], v[12:15], v[176:179], v[140:143]\n" \
    "v_mfma_f32_16x16x32_bf16 v[156:159], v[220:223], v[96:99], v[156:159]\n" "v_mfma_f32_16x16x32_bf16 v[144:147], v[44:47], v[176:179], v[144:147]\n" \
    "s_waitcnt lgkmcnt(5)\n" "v_mfma_f32_16x16x32_bf16 v[204:207], v[188:191], v[112:115], 0\n" \
    "v_mfma_f32_16x16x32_bf16 v[140:143], v[8:11], v[180:183], v[140:143]\n" "s_waitcnt lgkmcnt(3)\n" "v_cmp_gt_u32 vcc, 2, v234\n" \
    "s_cbranch_vccnz GMX_Su\n" "GMX_Ru:\n" "v_mfma_f32_16x16x32_bf16 v[208:211], v[188:191], v[116:119], 0\n" \
    "ds_read_b128 v[160:163], %[ub] offset:2048\n" "v_mfma_f32_16x16x32_bf16 v[144:147], v[40:43], v[180:183], v[144:147]\n" \
    "ds_read_b128 v[164:167], %[ub] offset:2112\n" "v_cvt_pk_bf16_f32 v212, v204, v205\n" \
    "v_mfma_f32_16x16x32_bf16 v[196:199], v[192:195], v[112:115], 0\n" "v_cvt_pk_bf16_f32 v213, v206, v207\n" \
    "ds_read_b128 v[168:171], %[ub] offset:2176\n" "s_waitcnt lgkmcnt(3)\n" "v_mfma_f32_16x16x32_bf16 v[140:143], v[4:7], v[184:187], v[140:143]\n" \
    "v_cvt_pk_bf16_f32 v214, v208, v209\n" "v_cvt_pk_bf16_f32 v215, v210, v211\n" "v_mfma_f32_16x16x32_bf16 v[200:203], v[192:195], v[116:119], 0\n" \
    "ds_read_b128 v[172:175], %[ub] offset:2240\n" "v_lshlrev_b32 v230, 16, v224\n" \
    "v_mfma_f32_16x16x32_bf16 v[144:147], v[36:39], v[184:187], v[144:147]\n" "ds_read_b128 v[176:179], %[ub] offset:2304\n" \
    "v_and_b32 v231, 0xffff0000, v224\n" "v_mfma_f32_16x16x32_bf16 v[204:207], v[188:191], v[120:123], 0\n" "v_lshlrev_b32 v232, 16, v225\n" \
    "v_and_b32 v233, 0xffff0000, v225\n" "v_mfma_f32_16x16x32_bf16 v[140:143], v[0:3], v[188:191], v[140:143]\n" \
    "ds_read_b128 v[180:183], %[ub] offset:2368\n" "v_cvt_pk_bf16_f32 v216, v196, v197\n" \
    "v_mfma_f32_16x16x32_bf16 v[208:211], v[192:195], v[124:127], 0\n" "v_cvt_pk_bf16_f32 v217, v198, v199\n" "v_cvt_pk_bf16_f32 v218, v200, v201\n" \
    "v_mfma_f32_16x16x32_bf16 v[144:147], v[32:35], v[188:191], v[144:147]\n" "v_cvt_pk_bf16_f32 v219, v202, v203\n" \
    "ds_read_b128 v[184:187], %[ub] offset:2432\n" "v_mfma_f32_16x16x32_bf16 v[148:151], v[64:67], v[188:191], v[148:151]\n" \
    "v_fmac_f32 v140, %[dm], v230\n" "v_fmac_f32 v141, %[dm], v231\n" "v_mfma_f32_16x16x32_bf16 v[156:159], v[108:111], v[188:191], v[156:159]\n" \
    PADL(103) "s_waitcnt lgkmcnt(4)\n" "v_mfma_f32_16x16x32_bf16 v[144:147], v[28:31], v[160:163], v[144:147]\n" "v_fmac_f32 v142, %[dm], v232\n" \
    "v_fmac_f32 v143, %[dm], v233\n" "v_mfma_f32_16x16x32_bf16 v[148:151], v[60:63], v[160:163], v[148:151]\n" "v_fmac_f32 v230, v226, v140\n" \
    "v_fmac_f32 v231, v227, v141\n" "v_mfma_f32_16x16x32_bf16 v[144:147], v[24:27], v[164:167], v[144:147]\n" "v_fmac_f32 v232, v228, v142\n" \
    "v_fmac_f32 v233, v229, v143\n" "v_mfma_f32_16x16x32_bf16 v[148:151], v[212:215], v[68:71], v[148:151]\n" \
    "ds_read_b128 v[188:191], %[ub] offset:2496\n" "v_cvt_pk_bf16_f32 v140, v230, v231\n" \
    "v_mfma_f32_16x16x32_bf16 v[148:151], v[56:59], v[164:167], v[148:151]\n" "v_cvt_pk_bf16_f32 v141, v232, v233\n" \
    "ds_write_b64 %[sb], v[140:141] offset:1536\n" "v_mfma_f32_16x16x32_bf16 v[152:155], v[212:215], v[72:75], v[152:155]\n" \
    "v_cvt_pk_bf16_f32 v220, v204, v205\n" "v_cvt_pk_bf16_f32 v221, v206, v207\n" \
    "v_mfma_f32_16x16x32_bf16 v[144:147], v[20:23], v[168:171], v[144:147]\n" "ds_write_b32 %[pg], %[pg] offset:96\n" \
    "v_cvt_pk_bf16_f32 v222, v208, v209\n" "v_mfma_f32_16x16x32_bf16 v[156:159], v[212:215], v[76:79], v[156:159]\n" \
    "ds_read_b128 v[192:195], %[ub] offset:2480\n" "v_cvt_pk_bf16_f32 v223, v210, v211\n" \
    "v_mfma_f32_16x16x32_bf16 v[148:151], v[52:55], v[168:171], v[148:151]\n" "ds_read_b128 v[160:163], %[ub] offset:2560\n" \
    "v_mfma_f32_16x16x32_bf16 v[148:151], v[216:219], v[80:83], v[148:151]\n" "ds_read_b128 v[164:167], %[ub] offset:2624\n" "s_waitcnt lgkmcnt(6)\n" \
    "v_mfma_f32_16x16x32_bf16 v[144:147], v[16:19], v[172:175], v[144:147]\n" "ds_read_b128 v[168:171], %[ub] offset:2688\n" \
    "v_mfma_f32_16x16x32_bf16 v[148:151], v[48:51], v[172:175], v[148:151]\n" "ds_read_b64 v[224:225], %[eb] offset:2048\n" \
    "v_mfma_f32_16x16x32_bf16 v[152:155], v[216:219], v[84:87], v[152:155]\n" "ds_read_b128 v[172:175], %[ub] offset:2752\n" \
    "v_mfma_f32_16x16x32_bf16 v[144:147], v[12:15], v[176:179], v[144:147]\n" "ds_read_b128 v[226:229], %[gb] offset:4096\n" \
    "v_mfma_f32_16x16x32_bf16 v[156:159], v[216:219], v[88:91], v[156:159]\n" "v_mfma_f32_16x16x32_bf16 v[148:151], v[44:47], v[176:179], v[148:151]\n" \
    "ds_read_b128 v[176:179], %[ub] offset:2816\n" "v_mfma_f32_16x16x32_bf16 v[156:159], v[220:223], v[92:95], v[156:159]\n" \
    "v_mfma_f32_16x16x32_bf16 v[144:147], v[8:11], v[180:183], v[144:147]\n" "s_waitcnt lgkmcnt(6)\n" \
    "v_mfma_f32_16x16x32_bf16 v[196:199], v[188:191], v[112:115], 0\n" "v_mfma_f32_16x16x32_bf16 v[148:151], v[40:43], v[180:183], v[148:151]\n" \
    "ds_read_b128 v[180:183], %[ub] offset:2880\n" "v_mfma_f32_16x16x32_bf16 v[144:147], v[4:7], v[184:187], v[144:147]\n" "s_waitcnt lgkmcnt(4)\n" \
    "v_lshlrev_b32 v230, 16, v224\n" "v_and_b32 v231, 0xffff0000, v224\n" "v_mfma_f32_16x16x32_bf16 v[200:203], v[188:191], v[116:119], 0\n" \
    "v_lshlrev_b32 v232, 16, v225\n" "v_cvt_pk_bf16_f32 v212, v196, v197\n" "v_mfma_f32_16x16x32_bf16 v[148:151], v[36:39], v[184:187], v[148:151]\n" \
    "v_cvt_pk_bf16_f32 v213, v198, v199\n" "v_and_b32 v233, 0xffff0000, v225\n" "v_mfma_f32_16x16x32_bf16 v[204:207], v[192:195], v[112:115], 0\n" \
    "ds_read_b128 v[184:187], %[ub] offset:2944\n" "v_mfma_f32_16x16x32_bf16 v[144:147], v[0:3], v[188:191], v[144:147]\n" \
    "v_cvt_pk_bf16_f32 v214, v200, v201\n" "v_cvt_pk_bf16_f32 v215, v202, v203\n" "v_mfma_f32_16x16x32_bf16 v[208:211], v[192:195], v[116:119], 0\n" \
    "ds_read_b128 v[192:195], %[ub] offset:2992\n" "v_mfma_f32_16x16x32_bf16 v[148:151], v[32:35], v[188:191], v[148:151]\n" \
    "ds_read_b64 v[224:225], %[eb] offset:2560\n" "v_cvt_pk_bf16_f32 v216, v204, v205\n" \
    "v_mfma_f32_16x16x32_bf16 v[152:155], v[64:67], v[188:191], v[152:155]\n" PADL(104) \
    "v_mfma_f32_16x16x32_bf16 v[148:151], v[28:31], v[160:163], v[148:151]\n" "ds_read_b128 v[188:191], %[ub] offset:3008\n" \
    "v_cvt_pk_bf16_f32 v217, v206, v207\n" "v_mfma_f32_16x16x32_bf16 v[152:155], v[60:63], v[160:163], v[152:155]\n" \
    "v_cvt_pk_bf16_f32 v218, v208, v209\n" "v_cvt_pk_bf16_f32 v219, v210, v211\n" \
    "v_mfma_f32_16x16x32_bf16 v[148:151], v[24:27], v[164:167], v[148:151]\n" "v_fmac_f32 v144, %[dm], v230\n" "v_fmac_f32 v145, %[dm], v231\n" \
    "v_mfma_f32_16x16x32_bf16 v[152:155], v[212:215], v[68:71], v[152:155]\n" "v_fmac_f32 v146, %[dm], v232\n" "v_fmac_f32 v147, %[dm], v233\n" \
    "v_mfma_f32_16x16x32_bf16 v[152:155], v[56:59], v[164:167], v[152:155]\n" "s_waitcnt lgkmcnt(3)\n" "v_fmac_f32 v230, v226, v144\n" \
    "v_fmac_f32 v231, v227, v145\n" "v_mfma_f32_16x16x32_bf16 v[148:151], v[20:23], v[168:171], v[148:151]\n" "v_fmac_f32 v232, v228, v146\n" \
    "v_fmac_f32 v233, v229, v147\n" "v_mfma_f32_16x16x32_bf16 v[156:159], v[212:215], v[72:75], v[156:159]\n" "v_cvt_pk_bf16_f32 v144, v230, v231\n" \
    "v_cvt_pk_bf16_f32 v145, v232, v233\n" "v_mfma_f32_16x16x32_bf16 v[152:155], v[52:55], v[168:171], v[152:155]\n" \
    "ds_write_b64 %[sb], v[144:145] offset:2048\n" "s_waitcnt lgkmcnt(2)\n" "v_lshlrev_b32 v230, 16, v224\n" \
    "v_mfma_f32_16x16x32_bf16 v[152:155], v[216:219], v[80:83], v[152:155]\n" "v_and_b32 v231, 0xffff0000, v224\n" "v_lshlrev_b32 v232, 16, v225\n" \
    "v_mfma_f32_16x16x32_bf16 v[148:151], v[16:19], v[172:175], v[148:151]\n" "ds_write_b32 %[pg], %[pg] offset:128\n" \
    "v_and_b32 v233, 0xffff0000, v225\n" "v_mfma_f32_16x16x32_bf16 v[152:155], v[48:51], v[172:175], v[152:155]\n" \
    "ds_read_b128 v[160:163], %[ub] offset:3072\n" "v_mfma_f32_16x16x32_bf16 v[156:159], v[216:219], v[84:87], v[156:159]\n" \
    "ds_read_b128 v[164:167], %[ub] offset:3136\n" "v_mfma_f32_16x16x32_bf16 v[148:151], v[12:15], v[176:179], v[148:151]\n" \
    "ds_read_b128 v[168:171], %[ub] offset:3200\n" "v_mfma_f32_16x16x32_bf16 v[152:155], v[44:47], v[176:179], v[152:155]\n" \
    "ds_read_b128 v[172:175], %[ub] offset:3264\n" "s_waitcnt lgkmcnt(6)\n" "v_mfma_f32_16x16x32_bf16 v[196:199], v[188:191], v[112:115], 0\n" \
    "ds_read_b128 v[176:179], %[ub] offset:3328\n" "v_mfma_f32_16x16x32_bf16 v[148:151], v[8:11], v[180:183], v[148:151]\n" \
    "ds_read_b128 v[226:229], %[gb] offset:5120\n" "v_mfma_f32_16x16x32_bf16 v[200:203], v[188:191], v[116:119], 0\n" \
    "ds_read_b64 v[224:225], %[eb] offset:3072\n" "v_mfma_f32_16x16x32_bf16 v[152:155], v[40:43], v[180:183], v[152:155]\n" \
    "ds_read_b128 v[180:183], %[ub] offset:3392\n" "v_mfma_f32_16x16x32_bf16 v[148:151], v[4:7], v[184:187], v[148:151]\n" \
    "v_cvt_pk_bf16_f32 v212, v196, v197\n" "v_cvt_pk_bf16_f32 v213, v198, v199\n" "v_mfma_f32_16x16x32_bf16 v[204:207], v[192:195], v[112:115], 0\n" \
    "v_mfma_f32_16x16x32_bf16 v[152:155], v[36:39], v[184:187], v[152:155]\n" "ds_read_b128 v[184:187], %[ub] offset:3456\n" \
    "v_cvt_pk_bf16_f32 v214, v200, v201\n" "v_mfma_f32_16x16x32_bf16 v[148:151], v[0:3], v[188:191], v[148:151]\n" \
    "v_cvt_pk_bf16_f32 v215, v202, v203\n" "v_mfma_f32_16x16x32_bf16 v[208:211], v[192:195], v[116:119], 0\n" \
    "v_mfma_f32_16x16x32_bf16 v[152:155], v[32:35], v[188:191], v[152:155]\n" "v_mfma_f32_16x16x32_bf16 v[156:159], v[64:67], v[188:191], v[156:159]\n" \
    PADL(105) "s_waitcnt lgkmcnt(3)\n" "v_mfma_f32_16x16x32_bf16 v[152:155], v[28:31], v[160:163], v[152:155]\n" \
    "ds_read_b128 v[188:191], %[ub] offset:3520\n" "v_fmac_f32 v148, %[dm], v230\n" \
    "v_mfma_f32_16x16x32_bf16 v[156:159], v[60:63], v[160:163], v[156:159]\n" "ds_read_b128 v[160:163], %[ub] offset:3584\n" \
    "v_fmac_f32 v149, %[dm], v231\n" "v_mfma_f32_16x16x32_bf16 v[152:155], v[24:27], v[164:167], v[152:155]\n" "v_fmac_f32 v150, %[dm], v232\n" \
    "v_fmac_f32 v151, %[dm], v233\n" "v_mfma_f32_16x16x32_bf16 v[156:159], v[56:59], v[164:167], v[156:159]\n" "v_fmac_f32 v230, v226, v148\n" \
    "v_fmac_f32 v231, v227, v149\n" "v_mfma_f32_16x16x32_bf16 v[152:155], v[20:23], v[168:171], v[152:155]\n" "v_fmac_f32 v232, v228, v150\n" \
    "v_fmac_f32 v233, v229, v151\n" "v_mfma_f32_16x16x32_bf16 v[156:159], v[52:55], v[168:171], v[156:159]\n" \
    "ds_read_b128 v[164:167], %[ub] offset:3648\n" "v_cvt_pk_bf16_f32 v148, v230, v231\n" \
    "v_mfma_f32_16x16x32_bf16 v[152:155], v[16:19], v[172:175], v[152:155]\n" "v_cvt_pk_bf16_f32 v149, v232, v233\n" \
    "ds_write_b64 %[sb], v[148:149] offset:2560\n" "v_mfma_f32_16x16x32_bf16 v[156:159], v[48:51], v[172:175], v[156:159]\n" \
    "v_cvt_pk_bf16_f32 v216, v204, v205\n" "v_cvt_pk_bf16_f32 v217, v206, v207\n" \
    "v_mfma_f32_16x16x32_bf16 v[152:155], v[12:15], v[176:179], v[152:155]\n" "ds_write_b32 %[pg], %[pg] offset:160\n" \
    "v_cvt_pk_bf16_f32 v218, v208, v209\n" "v_mfma_f32_16x16x32_bf16 v[156:159], v[212:215], v[68:71], v[156:159]\n" \
    "ds_read_b128 v[168:171], %[ub] offset:3712\n" "v_cvt_pk_bf16_f32 v219, v210, v211\n" \
    "v_mfma_f32_16x16x32_bf16 v[156:159], v[44:47], v[176:179], v[156:159]\n" "ds_read_b128 v[172:175], %[ub] offset:3776\n" "s_waitcnt lgkmcnt(5)\n" \
    "v_lshlrev_b32 v230, 16, v224\n" "v_mfma_f32_16x16x32_bf16 v[152:155], v[8:11], v[180:183], v[152:155]\n" \
    "ds_read_b128 v[176:179], %[ub] offset:3840\n" "v_and_b32 v231, 0xffff0000, v224\n" \
    "v_mfma_f32_16x16x32_bf16 v[156:159], v[40:43], v[180:183], v[156:159]\n" "ds_read_b128 v[180:183], %[ub] offset:3904\n" \
    "v_lshlrev_b32 v232, 16, v225\n" "v_mfma_f32_16x16x32_bf16 v[152:155], v[4:7], v[184:187], v[152:155]\n" \
    "ds_read_b128 v[226:229], %[gb] offset:6144\n" "v_and_b32 v233, 0xffff0000, v225\n" \
    "v_mfma_f32_16x16x32_bf16 v[156:159], v[36:39], v[184:187], v[156:159]\n" "ds_read_b128 v[184:187], %[ub] offset:3968\n" \
    "v_mfma_f32_16x16x32_bf16 v[152:155], v[0:3], v[188:191], v[152:155]\n" "ds_read_b64 v[224:225], %[eb] offset:3584\n" \
    "v_mfma_f32_16x16x32_bf16 v[156:159], v[32:35], v[188:191], v[156:159]\n" "ds_read_b128 v[188:191], %[ub] offset:4032\n" \
    "v_mfma_f32_16x16x32_bf16 v[156:159], v[216:219], v[80:83], v[156:159]\n" PADL(106) \
    "v_mfma_f32_16x16x32_bf16 v[156:159], v[28:31], v[160:163], v[156:159]\n" "s_waitcnt lgkmcnt(7)\n" \
    "v_mfma_f32_16x16x32_bf16 v[156:159], v[24:27], v[164:167], v[156:159]\n" "v_fmac_f32 v152, %[dm], v230\n" "v_fmac_f32 v153, %[dm], v231\n" \
    "v_mfma_f32_16x16x32_bf16 v[156:159], v[20:23], v[168:171], v[156:159]\n" "v_fmac_f32 v154, %[dm], v232\n" "v_fmac_f32 v155, %[dm], v233\n" \
    "s_waitcnt lgkmcnt(5)\n" "v_mfma_f32_16x16x32_bf16 v[156:159], v[16:19], v[172:175], v[156:159]\n" \
    "v_mfma_f32_16x16x32_bf16 v[156:159], v[12:15], v[176:179], v[156:159]\n" "s_waitcnt lgkmcnt(3)\n" "v_fmac_f32 v230, v226, v152\n" \
    "v_fmac_f32 v231, v227, v153\n" "v_mfma_f32_16x16x32_bf16 v[156:159], v[8:11], v[180:183], v[156:159]\n" "v_fmac_f32 v232, v228, v154\n" \
    "v_fmac_f32 v233, v229, v155\n" "s_waitcnt lgkmcnt(2)\n" "v_mfma_f32_16x16x32_bf16 v[156:159], v[4:7], v[184:187], v[156:159]\n" \
    "v_cvt_pk_bf16_f32 v152, v230, v231\n" "v_cvt_pk_bf16_f32 v153, v232, v233\n" "s_waitcnt lgkmcnt(0)\n" \
    "v_mfma_f32_16x16x32_bf16 v[156:159], v[0:3], v[188:191], v[156:159]\n" "ds_write_b64 %[sb], v[152:153] offset:3072\n" \
    "v_lshlrev_b32 v230, 16, v224\n" "ds_write_b32 %[pg], %[pg] offset:192\n" "ds_read_b128 v[226:229], %[gb] offset:7168\n" \
    "v_and_b32 v231, 0xffff0000, v224\n" "v_lshlrev_b32 v232, 16, v225\n" "v_and_b32 v233, 0xffff0000, v225\n" "s_nop 0\n" \
    "v_fmac_f32 v156, %[dm], v230\n" "v_fmac_f32 v157, %[dm], v231\n" "v_fmac_f32 v158, %[dm], v232\n" "v_fmac_f32 v159, %[dm], v233\n" \
    "s_waitcnt lgkmcnt(0)\n" "v_fmac_f32 v230, v226, v156\n" "v_fmac_f32 v231, v227, v157\n" "v_fmac_f32 v232, v228, v158\n" \
    "v_fmac_f32 v233, v229, v159\n" "v_cvt_pk_bf16_f32 v156, v230, v231\n" "v_cvt_pk_bf16_f32 v157, v232, v233\n" \
    "ds_write_b64 %[sb], v[156:157] offset:3584\n" "ds_write_b32 %[pg], %[pg] offset:224\n" "s_branch GMX_LE\n" "GMX_Sg:\n" "s_mov_b32 s80, 0x40000\n" \
    "GMX_Tg:\n" "s_sleep 1\n" "ds_read_b32 v234, %[pr] offset:64\n" "s_waitcnt lgkmcnt(0)\n" "v_cmp_gt_u32 vcc, 1, v234\n" "s_cbranch_vccz GMX_Ug\n" \
    "s_sub_u32 s80, s80, 1\n" "s_cbranch_scc0 GMX_Tg\n" "GMX_Ug:\n" "s_branch GMX_Rg\n" "GMX_Su:\n" "s_mov_b32 s80, 0x40000\n" "GMX_Tu:\n" \
    "s_sleep 1\n" "ds_read_b32 v234, %[pr] offset:32\n" "s_waitcnt lgkmcnt(0)\n" "v_cmp_gt_u32 vcc, 2, v234\n" "s_cbranch_vccz GMX_Uu\n" \
    "s_sub_u32 s80, s80, 1\n" "s_cbranch_scc0 GMX_Tu\n" "GMX_Uu:\n" "s_branch GMX_Ru\n" "GMX_LE:\n"

__global__ void __launch_bounds__(512, 2) __attribute__((aligned(4096)))
fused_kernel(const void*, const void*, const void*, void*, const void*, const void*, const void*, const void*,
             const void*, const void*, const void*, const void*, const void*, void*, const void*, const void*) {
    __shared__ __attribute__((aligned(16))) char smem[LDS_BYTES];
    const lptr lds = (lptr)smem;
    const int tid = threadIdx.x, lane = tid & 63;
    v8q ka0, ka1;
    u64 tstart64;
    asm volatile("s_memrealtime %2\n\ts_load_dwordx16 %0, %3, 0\n\ts_load_dwordx16 %1, %3, 64" : "=&s"(ka0), "=&s"(ka1), "=&s"(tstart64) : "s"(__builtin_amdgcn_kernarg_segment_ptr()));
    SCHED();
    const int wave = __builtin_amdgcn_readfirstlane(tid >> 6);
    const u32 lds0 = (u32)(uintptr_t)lds;
    {
        const u32 ln = umin((blockIdx.x & ~7u) + wave, CODE_LINES - 1);
        dma4s((gptr)__builtin_amdgcn_s_getpc() + ln * 128, 0, lds0 + DUMP_OFF, false);
    }
    lst<v4u>(lds + FL_OFF + lane * 16, (v4u){0u, 0u, 0u, 0u});
    int role = (blockIdx.x >> 5) & 1;
    const int i = lane & 15, g = lane >> 4;
    const int xcd = blockIdx.x & 7, jx = ((blockIdx.x >> 3) & 3) | ((blockIdx.x >> 6) << 2);
    const int pb = xcd * 16 + jx;
    const int ct = SAMEX ? (xcd * 14 + jx) >> 2 : (xcd >> 2) * 14 + jx, kq = SAMEX ? (xcd * 14 + jx) & 3 : xcd & 3;
    const bool has_w = jx < 14;
    const int ctu = ct + USHIFT - (ct + USHIFT >= 28 ? 28 : 0);
    u32 vo = (u32)(xcd * 256 + (g & 1) * 128 + (i & 3) * 16), xo0 = (u32)(((blockIdx.x >> 3) * 64 + 16 * (i >> 2) + 8 * (g >> 1)) * 2048) + vo;
    u32 duty = (pb & 63) == 14 ? 0x01111111u : (pb & 63) == 30 ? 0x8aaaaaaau : 0x0111aaaau;
    duty = has_w ? 0x0133aaaau : duty;
    duty = (role ? 0x0411aaaau : duty) >> (4 * wave);
    int kind = duty & 7, kg = duty & 8;
    const int ch = 8 * pb + wave + (role ? 0 : wave < 4 ? 4 : -128);
    u32 po0 = (u32)(ch * 256 + lane * 4), so0 = (u32)((ch >> 1) * 4);
    u32 ld0 = lds0 + W_OFF + wave * KROWS + (role * 4 + wave < 4 ? 131072 - 2 * KROWS : 0);
    asm volatile("" : "+v"(xo0), "+v"(vo), "+v"(po0), "+s"(ld0), "+v"(so0), "+s"(kind), "+s"(role), "+s"(kg));
    if (kind & 1) TOUCH_EARLY(wave);
    SCHED();
    asm volatile("s_waitcnt lgkmcnt(0)" : "+s"(ka0), "+s"(ka1), "+s"(tstart64) :: "memory");
    const u32 tstart = (u32)tstart64;
    const gptr x = (gptr)ka0[0], wr = (gptr)ka0[1], gw = (gptr)ka0[2], ws = (gptr)ka0[3], wg = (gptr)ka0[4], wu = (gptr)ka0[5], logdt = (gptr)ka0[6], dskip = (gptr)ka0[7];
    const gptr logA = (gptr)ka1[0], Aim = (gptr)ka1[1], Cre = (gptr)ka1[2], Cim = (gptr)ka1[3], wd = (gptr)ka1[4], out = (gptr)ka1[5];
    SCHED();
#define PARAMS() asm volatile("s_mov_b32 m0, %2\n\ts_nop 0\n\tglobal_load_lds_dword %0, %3 nt\n\ts_addk_i32 m0, 0x100\n\ts_nop 0\n\tglobal_load_lds_dword %1, %7\n\t"            \
                     "s_addk_i32 m0, 0x100\n\ts_nop 0\n\tglobal_load_lds_dword %0, %4 nt\n\ts_addk_i32 m0, 0x100\n\ts_nop 0\n\tglobal_load_lds_dword %0, %5 nt\n\t" \
                     "s_addk_i32 m0, 0x100\n\ts_nop 0\n\tglobal_load_lds_dword %0, %6 nt\n\ts_addk_i32 m0, 0x100\n\ts_nop 0\n\tglobal_load_lds_dword %1, %8"       \
                     :: "v"(po0), "v"(so0), "s"(ld0), "s"(logA), "s"(Aim), "s"(Cre), "s"(Cim), "s"(logdt), "s"(dskip) : "memory", "scc")
    u32 tag = 0;
    const __amdgpu_buffer_rsrc_t rs = MKRS(), rx = __builtin_amdgcn_make_buffer_rsrc((void*)x, 0, L_ * D_ * 2, 0x00020000);
    if (kg) PARAMS();
    if (kind == 4) {
#pragma unroll 1
        for (int p = 0; p < 7; p++) __builtin_amdgcn_raw_buffer_store_b128((v4u){~0u, ~0u, ~0u, ~0u}, rs, OFF_IN + pb * 7168 + lane * 16 + p * 1024, 0, SC1);
    }
    if (__builtin_expect(kind == 0, 1)) {
        v4u xa[16], wb[2];
        {
            const gptr vp = ((i & 4) ? gw : wr) + vo;
            wb[0] = *(const GAS v4u*)vp; wb[1] = *(const GAS v4u*)(vp + 64);
        }
        SCHED();
#define XROW(p) xa[p] = ((p) & 1) ? __builtin_amdgcn_raw_buffer_load_b128(rx, xo0 + 64 + ((p) & 2) * 1024, ((p) >> 2) * 4096, 0) : __builtin_amdgcn_raw_buffer_load_b128(rx, xo0 + ((p) & 2) * 1024, ((p) >> 2) * 4096, NT)
#pragma unroll
        for (int p = 0; p < 16; p++) {
            XROW(p);
            SCHED();
        }
        tag = EPOCH();
        int ro = OFF_P + ((((i >> 2) & 1) * 16 + (blockIdx.x >> 4)) * 8 + xcd) * 512 + ((blockIdx.x >> 3) & 1) * 256 + g * 64 + (i >> 3) * 32 + (i & 3) * 8;
        if (PREW) {
            __builtin_amdgcn_raw_buffer_store_b64((v2u){~0u, ~0u}, rs, ro, 0, SC1);
        }
        asm volatile("s_barrier" ::: "memory");
        __builtin_amdgcn_s_setprio(3);
        tag = MKTAG(tag);
        asm volatile("" : "+s"(tag), "+v"(ro));
        lst<u32>(lds + FL_OFF + F_TAGV * 4, tag); SETF(F_TAG, 1);
        STAMP(13);
        SCHED();
        if ((i >> 3) != (g >> 1)) wb[0] = wb[1] = (v4u){0, 0, 0, 0};
        SCHED();
        float s[2] = {0.f, 0.f}, v[4];
        f32x4 ac[8];
        u32 clv;
#define QSUM(h) do { \
            _Pragma("unroll") for (int t = 0; t < 4; t++) v[t] = __uint_as_float(DPP(__float_as_uint(ac[4 * (h) + t][0]), 0x00)); \
            SCHED(); _Pragma("unroll") for (int t = 0; t < 4; t++) v[t] = addd<0x55>(ac[4 * (h) + t][1], v[t]); \
            SCHED(); _Pragma("unroll") for (int t = 0; t < 4; t++) v[t] = addd<0xaa>(ac[4 * (h) + t][2], v[t]); \
            SCHED(); _Pragma("unroll") for (int t = 0; t < 4; t++) v[t] = addd<0xff>(ac[4 * (h) + t][3], v[t]); \
            SCHED(); _Pragma("unroll") for (int t = 0; t < 4; t++) s[t & 1] = (i & 3) == 2 * (h) + (t >> 1) ? v[t] : s[t & 1]; \
            SCHED(); } while (0)
#pragma unroll
        for (int p = 0; p < 16; p++) {
            ac[p >> 1] = mfma16(xa[p], wb[p & 1], (p & 1) ? ac[p >> 1] : (f32x4){0, 0, 0, 0});
            SCHED();
            if (p == 0) STAMP(14);
            if (p == 9) QSUM(0);
            if (p == 13) { clv = lld<u32>(lds + FL_OFF + F_CLR * 4); SCHED(); }
        }
        STAMP(15);
        const u32 clr = __builtin_amdgcn_readfirstlane(clv);
        QSUM(1);
        const v2u o = {tag, pk(s[0], s[1])};
        if (role && !clr) WAITF(F_CLR, 1);
        __builtin_amdgcn_raw_buffer_store_b64(o, rs, ro, 0, SC1);
        SETF(F_PUB, 1);
        STAMP(10);
    }
    asm volatile("" : "+s"(kind));
    if (kind != 0) {
        STAMP(12);
        asm volatile("s_barrier" ::: "memory");
        if (kind == 3) {
            const int lw = wave & 1;
            const u32 ro = (u32)(8 * (lane >> 5) + ((lane >> 3) & 3)), co = (u32)(((lane & 7) ^ ((lane >> 3) & 6)) << 4);
            gptr wp = (lw ? wu + 256 * ctu : wg + 256 * ct) + (u32)(256 * kq * 7168);
            u32 pitch = 7168, ld = lds0 + GU_OFF + lw * 2048, cnt = 0;
            STAMP(13);
#define GU_REQ(v, note) "s_waitcnt vmcnt(%9)\n\tglobal_load_lds_dwordx4 " v ", s[80:81] nt\n\ts_addk_i32 m0, 0x380\n\ts_waitcnt vmcnt(%9)\n\t" note     \
                        "global_load_lds_dwordx4 " v ", s[80:81] offset:128 nt\n\ts_addk_i32 m0, 0xc80\n\t"
#define GU_NOTE(w) ".if %9 == " #w "\n\tds_write_b32 %8, %2\n\tv_add_u32 %2, 1, %2\n\t.endif\n\t"
            static_assert(WIN == 10 || WIN == 12 || WIN == 14 || WIN == 16, "");
#pragma unroll 1
            for (int ph = 0; ph < 3; ph++) {
                if (ph == 1) waitf(lds + FL_OFF + (F_KG + (lane & 1)) * 4, 1);
                if (ph == 2) {
                    STAMP(16);
                    waitf(lds + FL_OFF + (F_FR + 2 * lw + (lane & 1)) * 4, 1);
                    wp = wd + (u32)(128 * ct * 2048 + 512 * kq + 256 * lw); pitch = 2048; ld -= 131072;
                }
                const u32 wo = ro * pitch + co;
                asm volatile("s_mov_b32 m0, %0\n\ts_add_i32 %0, %0, %10\n\ts_mov_b64 s[80:81], %1\n"
                             "GMX_GU:\n\t" GU_REQ("%3", GU_NOTE(9)) GU_REQ("%4", GU_NOTE(11)) GU_REQ("%5", GU_NOTE(13)) GU_REQ("%6", GU_NOTE(15))
                             "s_add_u32 s80, s80, %7\n\ts_addc_u32 s81, s81, 0\n\ts_cmp_lg_u32 m0, %0\n\ts_cbranch_scc1 GMX_GU\n\ts_mov_b64 %1, s[80:81]"
                             : "+s"(ld), "+s"(wp), "+v"(cnt) : "v"(wo), "v"(wo + 16 * pitch), "v"(wo + 4 * pitch), "v"(wo + 20 * pitch), "s"(32 * pitch),
                               "v"(lds0 + FL_OFF + (F_GU + lw) * 4), "i"(WIN - 1), "s"(((0x426 >> (4 * ph)) & 15) * 16384) : "memory", "scc", "s80", "s81");
            }
            STAMP(17);
            WAITV(8); SETF(F_GU + lw, 12);
            WAITV(0); SETF(F_GU + lw, 13);
            STAMP(18);
            STAMP_OUT();
        }
        if (kind == 4) { WAITV(0); SETF(F_CLR, 1); }
        if (kind == 1) {
#pragma unroll 1
            for (int t = role ? NPT / 2 - 4 + wave : has_w ? 0 : wave & 3; t < (role ? NPT / 2 + NTC : NPT / 2); t += role ? 2 : has_w ? 1 : 3) TOUCH_TAB(t);
        }
    }
    asm volatile("" : "+s"(role), "+s"(kg));

    const lptr wl = (lptr)ld0;
    const int r32 = lane & 31, h = lane >> 5;
    u32 kp[6], dsk = 0;
    const lptr tl = lds + T_OFF + wave * TP;
    lptr tn, t2, t3, pn, p2, ub, eb, sb, gb, pg, ma, pr;
#define CONV_SETUP() do { \
        tn = tl + (288 - 8 * r32 - 4 * h) * 4; \
        t2 = tl + T_P2 + (2 * (r32 & 1) * 80 + 52 - 16 * h - (r32 >> 1)) * 4 - 2; \
        t3 = tl + T_P3 + ((r32 & 1) * 96 + 52 - 32 * h - (r32 >> 1)) * 4 - 2; \
        pn = tl + (20 - i + 8 * g) * 4; p2 = tl + T_P2 + (20 - i + 4 * g) * 4; \
        ub = lds + U_OFF + wave * UP + (16 * i + 8 * g - 16) * 2 - 448; \
        eb = lds + U_OFF + wave * UP + (16 * i + 4 * g) * 2; \
        sb = lds + S_OFF + wave * UP + 32 * i + 8 * (g ^ (i >> 2)); \
        gb = lds + G_OFF + (16 * i + 4 * g) * 4; \
        pg = lds + PR_OFF + wave * 4; ma = lds + MSK_OFF + lane * 16; pr = lds + FL_OFF + (lane & 7) * 4; \
        asm volatile("" : "+v"(tn), "+v"(t2), "+v"(t3), "+v"(pn), "+v"(p2), "+v"(ub), "+v"(eb), "+v"(sb), "+v"(gb), "+v"(pg), "+v"(ma), "+v"(pr)); \
        lst<v2u>(lds + S_OFF + wave * UP + lane * 8, (v2u){0, 0}); \
        lst<v4u>(tl + T_P2 + lane * 16, (v4u){0, 0, 0, 0}); lst<v4u>(tl + T_P2 + 1024 + lane * 16, (v4u){0, 0, 0, 0}); \
        lst<u32>(tl + (lane < 32 ? lane : lane + 512) * 4, 0u); \
    } while (0)
    if (role && kg) { PAD(1); CONV_SETUP(); }
    if (kg) {
        PAD(2);
        lptr ea_, pa, qa;
        {
            const u64 c1 = 0x0d0c0b080b0a0904ull, c2 = 0x0f0e151413121110ull;
            const int erow = (r32 < 8 || r32 >= 24) ? (r32 & 7) : (int)(((r32 < 16 ? c1 : c2) >> (8 * (r32 & 7))) & 31u);
            ea_ = wl + erow * ROWB + h * 16;
            pa = wl + (r32 >> 2) * ROWB + h * 16; qa = wl + ((r32 & 3) ? 7 + (r32 & 3) : 0) * ROWB + h * 16;
            asm volatile("" : "+v"(ea_), "+v"(pa), "+v"(qa));
        }
        PAD(4);
        __builtin_amdgcn_s_setprio(2);
        WAITV(0);
        STAMP(1);
        const u32 pla = lld<u32>(wl + lane * 4), sld = lld<u32>(wl + 256 + lane * 4), pai = lld<u32>(wl + 512 + lane * 4);
        const u32 pcr = lld<u32>(wl + 768 + lane * 4), pci = lld<u32>(wl + 1024 + lane * 4), sds = lld<u32>(wl + 1280 + lane * 4);
        const float ldt = (wave & 1) ? bfhi(sld) : bflo(sld);
        dsk = sds;
        {
            const float dt = rbf(__expf(ldt));
            const u32 ea = pk(__expf(bflo(pla)), __expf(bfhi(pla)));
            const f32x2 l2 = (f32x2){bflo(ea) * dt, bfhi(ea) * dt} * -1.4426950408889634f;
            const f32x2 rev = (f32x2){bflo(pai) * dt, bfhi(pai) * dt} * -0.15915494309189535f;
            const u32 cm = umax(pcr & 0x7f807f80u, pci & 0x7f807f80u), cl = umax((pcr << 16) & 0x7f800000u, (pci << 16) & 0x7f800000u);
            const u32 ebits = umin(wave_max(umax(cm, cl)) & 0x7f800000u, 0x7e800000u);
            const float cinv = __uint_as_float(0x7f000000u - ebits), kscale = 2.f * dt * __uint_as_float(ebits);
            const c2 z1 = cpow(l2, rev), zB = cpow(l2 * 256.f, rev * 256.f);
            const c2 z2 = csq(z1), z4 = csq(z2);
            PAD(5);
            const lptr wp = wl + lane * 8;
#define PUTP(row, pv) lst<v2u>(wp + (row) * ROWB, pv)
#define PUT(row, v) PUTP(row, cpk(v))
            const c2 e0 = c2{(f32x2){bflo(pcr), bfhi(pcr)} * cinv, (f32x2){bflo(pci), bfhi(pci)} * -cinv};
            const c2 e4 = cmul(e0, z4), e2 = cmul(e0, z2);
            PUT(0, e0); PUT(9, cmul(e0, zB));
            SCHED();
            c2 t8 = cmul(e4, z4), t4c = cmul(e4, zB), q1 = csq(z4), q2, p1, p2_, p3_, p4;
            q1.i = -q1.i;
            PUT(4, e4); PUT(2, e2);
            SCHED();
            const c2 e6 = cmul(e4, z2);
            PUT(8, t8); PUT(11, t4c);
            SCHED();
#pragma unroll
            for (int st = 0; st < 7; st++) {
                PADV(6 + st, t8.r);
                t8 = cmul(t8, zB); PUT(st == 0 ? 13 : st == 1 ? 17 : st == 2 ? 21 : st < 5 ? 15 + st : 9 + st, t8);
                if (st < 2) { t4c = cmul(t4c, zB); PUT(st == 0 ? 16 : 20, t4c); }
                if (st == 0) PUT(6, e6);
                if (st == 2) { PUT(10, cmul(e2, zB)); PUT(12, cmul(e6, zB)); }
                if (st >= 3) PUT(2 * (st - 3) + 1, cmul(st == 3 ? e0 : st == 4 ? e2 : st == 5 ? e4 : e6, z1));
                if (st == 3) q2 = csq(q1);
                if (st == 4) p1 = csq(q2);
                if (st == 5) p2_ = csq(p1);
                if (st == 6) p4 = csq(p2_);
                SCHED();
            }
            STAMP(2);
            PAD(13);
            v4u ef[16];
#pragma unroll
            for (int s = 0; s < 16; s++) ef[s] = lld<v4u>(ea_ + s * 32);
            SCHED();
            p3_ = cmul(p2_, p1);
            PUTP(0, ((v2u){0x3c00u, 0x3c00u})); PUT(8, q1); PUT(9, q2); PUT(1, p1); PUT(2, p2_); PUT(4, p4);
            SCHED();
            PUT(10, cmul(q2, q1)); PUT(5, cmul(p4, p1)); PUT(3, p3_);
            SCHED();
            PUT(6, cmul(p4, p2_)); PUT(7, cmul(p4, p3_));
            SCHED();
            STAMP(3);
            PAD(14);
            f32x16 kd = {0};
            {
                v4u pv[16], qv[16], ff[16];
#pragma unroll
                for (int s = 0; s < 4; s++) { pv[s] = lld<v4u>(pa + s * 32); qv[s] = lld<v4u>(qa + s * 32); }
#define KSTEP(s) { \
                    if ((s) + 4 < 16) { pv[((s) + 4) & 15] = lld<v4u>(pa + ((s) + 4) * 32); qv[((s) + 4) & 15] = lld<v4u>(qa + ((s) + 4) * 32); } \
                    ff[s] = cmulh4(pv[s], qv[s]); \
                    SCHED(); \
                    if ((s) > 0) kd = __builtin_amdgcn_mfma_f32_32x32x16_f16(__builtin_bit_cast(h8_t, ef[((s) - 1) & 15]), __builtin_bit_cast(h8_t, ff[((s) - 1) & 15]), kd, 0, 0, 0); \
                    if ((s) == 5 || (s) == 11) PAD(15 + (s) / 6); \
                    SCHED(); \
                }
                KSTEP(0) KSTEP(1) KSTEP(2) KSTEP(3) KSTEP(4) KSTEP(5) KSTEP(6) KSTEP(7) KSTEP(8) KSTEP(9) KSTEP(10) KSTEP(11) KSTEP(12) KSTEP(13) KSTEP(14) KSTEP(15)
                asm volatile("s_nop 1" : "+v"(ff[15]));
                kd = __builtin_amdgcn_mfma_f32_32x32x16_f16(__builtin_bit_cast(h8_t, ef[15]), __builtin_bit_cast(h8_t, ff[15]), kd, 0, 0, 0);
            }
            STAMP(4);
#pragma unroll
            for (int r = 0; r < 4; r++) kp[r] = pk(kd[2 * r + 1] * kscale, kd[2 * r] * kscale);
            kp[4] = pk(kd[8] * kscale, kd[9] * kscale); kp[5] = pk(kd[10] * kscale, kd[11] * kscale);
        }
        if (!role) {
            WAITF(F_TAG, 1);
            SETF(F_KG + wave, 1);
            tag = __builtin_amdgcn_readfirstlane(lld<u32>(lds + FL_OFF + F_TAGV * 4));
#pragma unroll
            for (int r = 0; r < 3; r++) __builtin_amdgcn_raw_buffer_store_b128((v4u){tag, kp[2 * r], tag, kp[2 * r + 1]}, rs, OFF_KT + ((ch >> 3) * 4 + (ch & 3)) * KTP + r * 1024 + lane * 16, 0, SC1);
            STAMP(11);
        }
    }
    asm volatile("" : "+s"(role));

    if (!role) {
        PAD(58);
        const int lane = lane_id(), i = lane & 15, g = lane >> 4;
        if (__builtin_expect(!has_w, 0)) {
            if (jx & 1) {
                if (wave < 4) END();
                PAD(69);
                WAITF(F_PUB, 1);
#pragma unroll 1
                for (int j = wave & 3; j < NPULL && wave < 7; j += 3) {
                    const int o = xcd * 14 + j, ct2 = SAMEX ? o >> 2 : (xcd >> 2) * 14 + j, kq2 = SAMEX ? o & 3 : kq;
                    dma4s(wd, (u32)((128 * ct2 + lane) * 2048 + 512 * kq2), lds0 + DUMP_OFF, false);
                    WAITV(0);
                }
                STAMP(21);
                STAMP_OUT();
                END();
            }
            const bool gates = xcd & 2;
            if (gates ? wave < 4 : (xcd & 1) != 0) END();
            PAD(61);
            __builtin_amdgcn_s_setprio(3);
            const int rho = (xcd & 4) * 2 + (wave ^ ((xcd & 1) * 4)), r2 = 2 * (lane & 31) + (lane >> 5);
            u32 lo = (u32)(2047 - 128 * rho - 2 * r2);
            int po = OFF_P + (gates * 16 + rho) * 4096 + lane * 16, oo = gates ? OFF_G + rho * 512 + r2 * 8 : OFF_F + (lane >> 3) * 256 + (xcd >> 2) * 128 + (lane & 7) * 16;
            asm volatile("" : "+v"(lo), "+v"(po), "+v"(oo));
            if (PREW && (gates || wave == 0)) {
                if (gates) __builtin_amdgcn_raw_buffer_store_b64((v2u){~0u, ~0u}, rs, oo, 0, SC1);
                else __builtin_amdgcn_raw_buffer_store_b128((v4u){~0u, ~0u, ~0u, ~0u}, rs, oo, 0, SC1);
            }
            WAITF(F_PUB, 1);
            tag = __builtin_amdgcn_readfirstlane(lld<u32>(lds + FL_OFF + F_TAGV * 4));
            if (PTIME) __builtin_amdgcn_s_sleep(PTIME);
            STAMP(16);
            u32 p01 = dots<64>(rs, po, tag);
            STAMP(17);
            if (gates) {
                __builtin_amdgcn_raw_buffer_store_b64((v2u){tag, pk(sigm(bflo(p01)), sigm(bfhi(p01)))}, rs, oo, 0, SC1);
                STAMP(19);
                STAMP_OUT();
                END();
            }
            p01 ^= 0x80008000u | (((p01 >> 15) & 0x10001u) * 0x7fffu);
            u32 ka = (p01 << 16) | lo, kb = (p01 & 0xffff0000u) | (lo - 1u);
            PADV(65, ka);
            top8<true>(ka, kb, lane);
            if (lane < 4) lst<v2u>(lds + KEY_OFF + wave * 32 + lane * 8, (v2u){ka, kb});
            CFENCE();
            STAMP(18);
            PAD(66);
            const u32 nth = __builtin_amdgcn_readfirstlane(lane == 0 ? __hip_atomic_fetch_add((LAS u32*)(lds + FL_OFF + F_XS * 4), 1u, __ATOMIC_RELAXED, __HIP_MEMORY_SCOPE_WORKGROUP) : 0u);
            const v2u kk = lld<v2u>(lds + KEY_OFF + (lane & 31) * 8);
            if (nth == 7) {
                ka = kk.x; kb = kk.y;
                top8<false>(ka, kb, lane);
                __builtin_amdgcn_raw_buffer_store_b128((v4u){tag, ka, tag, kb}, rs, oo, 0, SC1);
                __builtin_amdgcn_raw_buffer_store_b128((v4u){tag, ka, tag, kb}, rs, oo + (OFF_F2 - OFF_F), 0, SC1);
                STAMP(20);
            }
            STAMP_OUT();
            END();
        }
        if (wave >= 4) {
            if (wave != 7) END();
            PAD(62);
            const int fo = OFF_F + xcd * 256;
            u32 xs = (u32)(2047 * 2048 + 512 * kq + (((lane & 7) ^ ((lane >> 3) & 6)) << 4)), key, miss;
            asm volatile("" : "+v"(xs));
            if (RTIME) { while ((u32)__builtin_amdgcn_s_memrealtime() - tstart < (u32)RTIME) __builtin_amdgcn_s_sleep(1); }
            STAMP(18);
            asm volatile("s_movk_i32 s80, 64\n"
                         "GMX_FP:\n\ts_load_dwordx16 s[48:63], %2, 0 glc\n\ts_load_dwordx16 s[64:79], %2, 128 glc\n\ts_waitcnt lgkmcnt(0)\n\t"
                         "s_xor_b32 s48, s48, %3\n\t""s_xor_b32 s50, s50, %3\n\t""s_xor_b32 s52, s52, %3\n\t""s_xor_b32 s54, s54, %3\n\t""s_xor_b32 s56, s56, %3\n\t""s_xor_b32 s58, s58, %3\n\t""s_xor_b32 s60, s60, %3\n\t""s_xor_b32 s62, s62, %3\n\t"
                         "s_xor_b32 s64, s64, %3\n\t""s_xor_b32 s66, s66, %3\n\t""s_xor_b32 s68, s68, %3\n\t""s_xor_b32 s70, s70, %3\n\t""s_xor_b32 s72, s72, %3\n\t""s_xor_b32 s74, s74, %3\n\t""s_xor_b32 s76, s76, %3\n\t""s_xor_b32 s78, s78, %3\n\t"
                         "s_or_b32 s48, s48, s50\n\t""s_or_b32 s52, s52, s54\n\t""s_or_b32 s56, s56, s58\n\t""s_or_b32 s60, s60, s62\n\t""s_or_b32 s64, s64, s66\n\t""s_or_b32 s68, s68, s70\n\t""s_or_b32 s72, s72, s74\n\t""s_or_b32 s76, s76, s78\n\t"
                         "s_or_b32 s48, s48, s52\n\t""s_or_b32 s56, s56, s60\n\t""s_or_b32 s64, s64, s68\n\t""s_or_b32 s72, s72, s76\n\t"
                         "s_or_b32 s48, s48, s56\n\t""s_or_b32 s64, s64, s72\n\t"
                         "s_or_b32 %1, s48, s64\n\ts_cbranch_scc0 GMX_FD\n\t"
                         "s_sleep 1\n\ts_addk_i32 s80, -1\n\ts_cmp_lg_u32 s80, 0\n\ts_cbranch_scc1 GMX_FP\n"
                         "GMX_FD:\n\t""s_max_u32 s49, s49, s79\n\t""s_max_u32 s51, s51, s77\n\t""s_max_u32 s53, s53, s75\n\t""s_max_u32 s55, s55, s73\n\t""s_max_u32 s57, s57, s71\n\t""s_max_u32 s59, s59, s69\n\t""s_max_u32 s61, s61, s67\n\t""s_max_u32 s63, s63, s65\n\t"
                         "v_mov_b32 %0, s49\n\ts_lshl_b64 exec, exec, 8\n\t""v_mov_b32 %0, s51\n\ts_lshl_b64 exec, exec, 8\n\t""v_mov_b32 %0, s53\n\ts_lshl_b64 exec, exec, 8\n\t""v_mov_b32 %0, s55\n\ts_lshl_b64 exec, exec, 8\n\t"
                         "v_mov_b32 %0, s57\n\ts_lshl_b64 exec, exec, 8\n\t""v_mov_b32 %0, s59\n\ts_lshl_b64 exec, exec, 8\n\t""v_mov_b32 %0, s61\n\ts_lshl_b64 exec, exec, 8\n\t""v_mov_b32 %0, s63\n\ts_mov_b64 exec, -1"
                         : "=&v"(key), "=&s"(miss) : "s"(ws + fo), "s"(tag)
                         : "memory", "scc", "s48", "s49", "s50", "s51", "s52", "s53", "s54", "s55", "s56", "s57", "s58", "s59", "s60", "s61", "s62", "s63", "s64", "s65", "s66", "s67", "s68", "s69", "s70", "s71", "s72", "s73", "s74", "s75", "s76", "s77", "s78", "s79", "s80");
            if (__builtin_expect(miss != 0u, 0)) {
                COLD_BEGIN();
#pragma unroll 1
                for (int spins = 0; spins < SPIN_CAP; ++spins) {
                    CFENCE();
                    const v2u q = __builtin_amdgcn_raw_buffer_load_b64(rs, fo + (lane >> 3) * 8, 0, SC1), r = __builtin_amdgcn_raw_buffer_load_b64(rs, fo + 184 - (lane >> 3) * 8, 0, SC1);
                    key = umax(q.y, r.y);
                    if (__all((q.x == tag) & (r.x == tag))) break;
                }
                COLD_END();
            }
            STAMP(19);
            xs -= (key & 2047u) * 2048u;
#pragma unroll
            for (int p = 0; p < 4; p++) dma16cs(x + 128 * p, xs, lds0 + XS_OFF + p * 1024);
            SCHED();
            lst<u32>(lds + KEY_OFF + (lane >> 3) * 4, key);
            if (pb == 0 && lane == 0) __builtin_amdgcn_raw_buffer_store_b32(EPOCH() + 1u, rs, OFF_E, 0, SC1);
            WAITV(0);
            SETF(F_XS, 1);
            STAMP(5);
            STAMP_OUT();
            END();
        }
        PAD(40);
        __builtin_amdgcn_s_setprio(3);
        const int un = wave & 3, hi = i >> 3, row = 4 * (g & 1) + (i >> 2);
        int po = OFF_H2 + ((un & 2) ? ctu : ct) * 16384 + (((un & 1) * 2 + (lane >> 5)) * 4 + kq) * 1024 + (un >> 1) * 512 + (lane & 31) * 16;
        int ho = OFF_H3 + (32 * kq + 8 * un + 4 * hi + (g >> 1)) * 3584 + ct * 128 + (i & 7) * 16 + (g & 1) * 8;
        lptr gp[4], fl = lds + FL_OFF + (F_GU + (un >> 1)) * 4;
#pragma unroll
        for (int e = 0; e < 4; e++) gp[e] = lds + GU_OFF + un * 1024 + (g >> 1) * 4096 + row * 128 + ((e ^ (row >> 1)) << 5) + (i & 3) * 8;
        f32x4 acc[4];
        v4u wf[8][4], xb[8];
#define RDW(s) _Pragma("unroll") for (int e = 0; e < 4; e++) { const v2u lo = ldtr(gp[e] + (s) * 16384), hh = ldtr(gp[e] + (s) * 16384 + 8192); wf[s][e] = (v4u){lo.x, lo.y, hh.x, hh.y}; }
#define MMW(s) _Pragma("unroll") for (int e = 0; e < 4; e++) acc[e] = mfma16(wf[s][e], xb[s], (s) ? acc[e] : (f32x4){0, 0, 0, 0});
        asm volatile("" : "+v"(po), "+v"(ho), "+v"(fl));
        waitf(fl, 5);
        RDW(0) RDW(1) RDW(2) RDW(3)
        SETF(F_FR + un, 1);
        PAD(42);
        waitf(fl, 8);
        RDW(4) RDW(5) RDW(6)
        STAMP(14);
        bool late = true;
#pragma unroll 1
        for (int spins = 0; spins < LSPIN_CAP; ++spins) {
            CFENCE();
            const u32 a = __builtin_amdgcn_readfirstlane(lld<u32>(fl)), b = __builtin_amdgcn_readfirstlane(lld<u32>(lds + FL_OFF + F_XS * 4));
            late = a < 9;
            if (!late || b) break;
            __builtin_amdgcn_s_sleep(1);
        }
        CFENCE();
        if (!late) { RDW(7) WAITF(F_XS, 1); }
        STAMP(15);
        PAD(43);
#pragma unroll
        for (int s = 0; s < 8; s++) xb[s] = lld<v4u>(lds + XS_OFF + (s >> 1) * 1024 + (i & 7) * 128 + (((4 * (s & 1) + g) ^ (i & 6)) << 4));
        const u32 sk = lld<u32>(lds + KEY_OFF + (i & 7) * 4) >> 16;
        SCHED();
#pragma unroll
        for (int s = 0; s < 7; s++) MMW(s)
        SCHED();
        if (late) { waitf(fl, 9); RDW(7) }
        MMW(7)
        PAD(44);
        {
            f32x4 a0, a1;
#pragma unroll
            for (int r = 0; r < 4; r++) { a0[r] = hi ? acc[2][r] : acc[0][r]; a1[r] = hi ? acc[3][r] : acc[1][r]; }
            __builtin_amdgcn_raw_buffer_store_b128((v4u){pkd(a0[0], a0[1]), pkd(a0[2], a0[3]), pkd(a1[0], a1[1]), pkd(a1[2], a1[3])}, rs, po, 0, SC1);
        }
        STAMP(7);
        float rsel = rbf(sigm(__uint_as_float(((sk & 0x8000u) ? (sk ^ 0x8000u) : (sk ^ 0xffffu)) << 16)));
        asm volatile("" : "+v"(rsel));
        CFENCE();
        late = __builtin_amdgcn_readfirstlane(lld<u32>(fl)) < 13;
        CFENCE();
        if (!late) { RDW(0) RDW(1) RDW(2) RDW(3) }
        if (H2DELAY) __builtin_amdgcn_s_sleep(H2DELAY);
        {
            v4u q[4];
            fetch<4, 1024, false, 83>(q, rs, OFF_H2 + ct * 16384 + un * 4096 + lane * 16, 0);
            STAMP(6);
            float hv[4];
            f32x2 sm[4];
#pragma unroll
            for (int d = 0; d < 4; d++) sm[d] = ((f32x2){bflo(q[0][d]), bfhi(q[0][d])} + (f32x2){bflo(q[1][d]), bfhi(q[1][d])}) + ((f32x2){bflo(q[2][d]), bfhi(q[2][d])} + (f32x2){bflo(q[3][d]), bfhi(q[3][d])});
            PADV(85, sm[0]);
#pragma unroll
            for (int r = 0; r < 4; r++) {
                const auto sw = __builtin_amdgcn_permlane32_swap(__float_as_uint(sm[r >> 1][r & 1]), __float_as_uint(sm[2 + (r >> 1)][r & 1]), false, false);
                const float gr = rbf(__uint_as_float(sw[0]));
                hv[r] = rbf(gr * sigm(gr)) * rbf(__uint_as_float(sw[1]));
            }
            lst<v2u>(lds + HS_OFF + (i & 7) * 272 + (un >> 1) * 128 + hi * 64 + (lane >> 5) * 32 + (un & 1) * 16 + (g & 1) * 8, (v2u){pk(hv[0], hv[1]), pk(hv[2], hv[3])});
            SETF(F_H + un, 1);
        }
        PAD(86);
        if (late) { waitf(fl, 13); RDW(0) RDW(1) RDW(2) RDW(3) }
        PAD(87);
        waitf(lds + FL_OFF + (F_H + (lane & 3)) * 4, 1);
        STAMP(8);
#pragma unroll
        for (int s = 0; s < 4; s++) xb[s] = lld<v4u>(lds + HS_OFF + (i & 7) * 272 + 64 * s + 16 * g);
        MMW(0) MMW(1) MMW(2) MMW(3)
#pragma unroll
        for (int e = 0; e < 2; e++) {
            f32x4 v;
#pragma unroll
            for (int r = 0; r < 4; r++) v[r] = (hi ? acc[2 + e][r] : acc[e][r]) * rsel;
            __builtin_amdgcn_raw_buffer_store_b64((v2u){pkd(v[0], v[1]), pkd(v[2], v[3])}, rs, ho + e * 7168, 0, SC1);
        }
        STAMP(9);
        STAMP_OUT();
        END();
    }

    PAD(0);
    const bool isb = wave >= 4;
    if (isb) {
        PAD(30);
        const int wq = wave & 3;
        dsk = *(const __attribute__((address_space(4))) u32*)(u64)(dskip + (ch >> 1) * 4);
        u32 xo = (u32)((wq * 128 + lane) * 8192 + pb * 16);
        asm volatile("" : "+v"(xo));
        if (wq < 2) __builtin_amdgcn_s_setprio(3); else __builtin_amdgcn_s_setprio(1);
#pragma unroll 1
        for (int m = wq; m < 5 && wq < 2; m += 2) lst<v4u>(lds + MSK_OFF + m * 1024 + lane * 16, group_mask(m, lane));
        WAITF(F_PUB, 1);
        tag = __builtin_amdgcn_readfirstlane(lld<u32>(lds + FL_OFF + F_TAGV * 4));
        if (wq >= 2) __builtin_amdgcn_s_sleep(XDELAY);
        STAMP(1);
        v4u xc[8], gq[4], kq3[3];
#pragma unroll
        for (int k = 0; k < 8; k++) { xc[k] = __builtin_amdgcn_raw_buffer_load_b128(rx, xo + (k & 1) * 2048, (k >> 2) * 524288 + (k & 2) * 2048, NT); SCHED(); }
        STAMP(2);
        PAD(31);
#pragma unroll
        for (int t = 0; t < 2; t++)
#pragma unroll
            for (int e = 0; e < 8; e++) {
                const u32 sel = (e & 1) ? 0x07060302u : 0x05040100u;
                lst<v2u>(lds + e * UP + U_OFF + t * 512 + (wq * 128 + lane) * 8, (v2u){__builtin_amdgcn_perm(xc[4 * t + 1][e >> 1], xc[4 * t][e >> 1], sel), __builtin_amdgcn_perm(xc[4 * t + 3][e >> 1], xc[4 * t + 2][e >> 1], sel)});
            }
        SETF(F_UG + wq + (lane & 4), 2);
        STAMP(5);
        __builtin_amdgcn_s_setprio(1);
        CONV_SETUP();
        SCHED();
        PAD(32);
        if (wq < 2) {
            while ((u32)__builtin_amdgcn_s_memrealtime() - tstart < (u32)GTIME) __builtin_amdgcn_s_sleep(1);
            STAMP(3);
            fetch<4, 1024, true, 24>(gq, rs, OFF_G + wq * 4096 + lane * 16, tag);
#pragma unroll
            for (int t = 0; t < 4; t++) lst<f32x4>(lds + G_OFF + wq * 4096 + t * 1024 + lane * 16, (f32x4){bflo(gq[t].y), bfhi(gq[t].y), bflo(gq[t].w), bfhi(gq[t].w)});
            SETF(F_GT + wq + (lane & 6), 1);
            STAMP(16);
        }
        fetch<3, 1024, true, 26>(kq3, rs, OFF_KT + (pb * 4 + wq) * KTP + lane * 16, tag);
        if (CPULL == 1 && has_w && wave != 7) {
            dma4s(wd, (u32)((128 * ct + 64 + lane) * 2048 + 512 * kq + 128 * wq), lds0 + DUMP_OFF, false);
            if (wq == 0) dma4s(wd, (u32)((128 * ct + 64 + lane) * 2048 + 512 * kq + 384), lds0 + DUMP_OFF, false);
        }
#pragma unroll
        for (int r = 0; r < 3; r++) { kp[2 * r] = kq3[r].y; kp[2 * r + 1] = kq3[r].w; }
        STAMP(4);
        if (wave == 7) {
            const int fo = OFF_F2 + xcd * 256 + (lane & 7) * 8;
            v2u fq, fr;
            while ((u32)__builtin_amdgcn_s_memrealtime() - tstart < (u32)FTIME) __builtin_amdgcn_s_sleep(2);
#pragma unroll 1
            for (int spins = 0; spins < SPIN_CAP; ++spins) {
                PAD(27);
                CFENCE();
                fq = __builtin_amdgcn_raw_buffer_load_b64(rs, fo, 0, SC1); fr = __builtin_amdgcn_raw_buffer_load_b64(rs, fo ^ 184, 0, SC1);
                if (__all((fq.x == tag) & (fr.x == tag))) break;
                __builtin_amdgcn_s_sleep(2);
            }
            const u32 rrow = 2047u - (umax(fq.y, fr.y) & 2047u);
            if (lane < 8) {
                __hip_atomic_fetch_or((LAS u32*)(lds + SKIP_OFF + (rrow >> 5) * 4), 1u << (rrow & 31), __ATOMIC_RELAXED, __HIP_MEMORY_SCOPE_WORKGROUP);
                lst<u32>(lds + SKIP_OFF + 256 + lane * 4, rrow);
            }
            SETF(F_SKIP, 1);
            STAMP(17);
        }
    }
    PAD(17);
    {
        lst<v4u>(tn + 1024, (v4u){__builtin_amdgcn_alignbit(kp[1], kp[2], 16), kp[1], __builtin_amdgcn_alignbit(kp[0], kp[1], 16), kp[0]});
        if (lane == 0) lst<u32>(tl + 548 * 4, kp[0] >> 16);
        const u32 k77 = __builtin_amdgcn_perm(kp[3], kp[3], 0x01000100u);
        lst<v4u>(tn, (v4u){k77, kp[3], __builtin_amdgcn_alignbit(kp[2], kp[3], 16), __builtin_amdgcn_perm(kp[2], kp[2], 0x01000100u)});
        if (lane == 63) lst<v4u>(tl + 32 * 4, (v4u){0u, k77 & 0xffff0000u, k77, k77});
        const u32 s2 = kp[4], s3 = kp[5];
        lst<u16>(t2 + 2, (u16)s2); lst<u16>(t2 + 322, (u16)(s2 >> 16)); lst<u16>(t3 + 66, (u16)s3); lst<u16>(t3 + 2, (u16)(s3 >> 16));
        CFENCE();
        lst<u16>(t2, (u16)s2); lst<u16>(t2 + 320, (u16)(s2 >> 16)); lst<u16>(t3 + 64, (u16)s3); lst<u16>(t3, (u16)(s3 >> 16));
    }
    const float dm1 = __uint_as_float(__builtin_amdgcn_readfirstlane(__float_as_uint(((wave & 1) ? bfhi(dsk) : bflo(dsk)) - 1.f)));
    STAMP(6);
    waitf((lptr)((u32)(uintptr_t)pr & ~24u) + F_UG * 4, 2);
    PAD(23);
    STAMP(8); STAMPC(22);
    asm volatile("s_waitcnt lgkmcnt(0)\n" CONV_LOOP :: [ub] "v"(ub), [eb] "v"(eb), [sb] "v"(sb), [gb] "v"(gb), [pg] "v"(pg), [dm] "s"(dm1), [pn] "v"(pn), [p2] "v"(p2),
                 [ma] "v"(ma), [pr] "v"(pr), [k1] "s"(tl + T_P2 + (3 * 80 + 84 - 63) * 4) LOOP_BRKS : LOOP_CLOB);
    STAMP(9); STAMPC(23);
    PAD(140);
    if (isb) { STAMP(11); STAMP_OUT(); END(); }
    const int ln = lane_id();
    if (wave != 0) {
        const lptr ob = lds + S_OFF + ((ln >> 2) & 3) * UP + 32 * (ln >> 4), pf = lds + PR_OFF + (ln & 7) * 4;
        WAITF(F_SKIP, 1);
#pragma unroll 1
        for (int c = wave - 1; c < 32; c += 3) {
            const u32 sk = lld<u32>(lds + SKIP_OFF + c * 8 + (ln >> 5) * 4);
            waitf(pf + (c >> 2) * 32, 1);
            const lptr o = ob + 128 * c + 8 * ((ln & 3) ^ (c & 3));
            const v2u a0 = ldtr(o), a1 = ldtr(o + 4 * UP);
            if (!((sk >> (ln & 31)) & 1u)) *(GAS v4u*)(out + (u32)((64 * c + ln) * 2048 + 16 * pb)) = (v4u){a0.x, a0.y, a1.x, a1.y};
        }
        STAMP(11);
        STAMP_OUT();
        END();
    }
    PAD(141);
    WAITF(F_SKIP, 1);
    const u32 rrow = lld<u32>(lds + SKIP_OFF + 256 + (ln & 7) * 4);
    {
        const int ho = OFF_H3 + pb * 3584 + ln * 16, h3 = ln < 32 ? ho + 3072 : WS_USED;
        v4u q[4];
        const float omg = 1.f - lld<float>(lds + G_OFF + rrow * 4);
        while ((u32)__builtin_amdgcn_s_memrealtime() - tstart < (u32)HTIME) __builtin_amdgcn_s_sleep(2);
        bool ok = false;
#pragma unroll 1
        for (int spins = 0; spins < SPIN_CAP; ++spins) {
            PAD(142);
            CFENCE();
            if (!ok) {
#pragma unroll
                for (int k = 0; k < 3; k++) q[k] = __builtin_amdgcn_raw_buffer_load_b128(rs, ho, k * 1024, SC1);
                q[3] = __builtin_amdgcn_raw_buffer_load_b128(rs, h3, 0, SC1);
            }
            ok = arrived<4, false>(q, 0);
            if (__all(ok)) break;
            if (PSLEEP) __builtin_amdgcn_s_sleep(PSLEEP);
        }
        STAMP(10);
        PAD(143);
        f32x2 y[4];
#pragma unroll
        for (int d = 0; d < 4; d++) y[d] = ((f32x2){bflo(q[0][d]), bfhi(q[0][d])} + (f32x2){bflo(q[1][d]), bfhi(q[1][d])}) + ((f32x2){bflo(q[2][d]), bfhi(q[2][d])} + (f32x2){bflo(q[3][d]), bfhi(q[3][d])});
        float z[4], z2[2];
#pragma unroll
        for (int d = 0; d < 4; d++) {
            const auto sw = __builtin_amdgcn_permlane32_swap(__float_as_uint(y[d >> 1][d & 1]), __float_as_uint(y[2 + (d >> 1)][d & 1]), false, false);
            z[d] = __uint_as_float(sw[0]) + __uint_as_float(sw[1]);
        }
#pragma unroll
        for (int d = 0; d < 2; d++) {
            const auto sw = __builtin_amdgcn_permlane16_swap(__float_as_uint(z[d]), __float_as_uint(z[2 + d]), false, false);
            z2[d] = __uint_as_float(sw[0]) + __uint_as_float(sw[1]);
        }
        const float ys = ((ln & 8) ? z2[1] : z2[0]) + __uint_as_float(DPP(__float_as_uint((ln & 8) ? z2[0] : z2[1]), 0x128));
        const int e = ln >> 3;
        waitf(lds + PR_OFF + (rrow >> 8) * 32 + e * 4, 1);
        const float base = __uint_as_float((u32)lld<u16>(lds + S_OFF + e * UP + ((rrow * 2) ^ ((rrow >> 3) & 24))) << 16);
        const float o = base + omg * ys;
        *(GAS u16*)(out + (rrow * 2048 + 16 * pb + 2 * e)) = (u16)pk(o, o);
    }
    STAMP(11);
    STAMP_OUT();
    END();
}

extern "C" void kernel_launch(void* const* d_in, const int* in_sizes, int n_in, void* d_out, int out_size, void* d_ws,
                              size_t ws_size, hipStream_t stream) {
    fused_kernel<<<256, 512, 0, stream>>>(d_in[0], d_in[8], d_in[12], (char*)d_ws + WS_BASE, d_in[9], d_in[10], d_in[5], d_in[6],
                                          d_in[1], d_in[2], d_in[3], d_in[4], d_in[11], d_out, d_in[0], d_in[0]);
}
```

```cpp
#include <hip/hip_runtime.h>
#include <stdint.h>

typedef unsigned short u16;
typedef unsigned u32;
typedef unsigned long long u64;
typedef u32 v4u __attribute__((ext_vector_type(4)));
typedef u32 v2u __attribute__((ext_vector_type(2)));
typedef float f32x2 __attribute__((ext_vector_type(2)));
typedef float f32x4 __attribute__((ext_vector_type(4)));
typedef float f32x16 __attribute__((ext_vector_type(16)));
typedef short bf16x8 __attribute__((ext_vector_type(8)));
typedef short s16x4 __attribute__((ext_vector_type(4)));
typedef __bf16 bf2_t __attribute__((ext_vector_type(2)));
#define LAS __attribute__((address_space(3)))
typedef LAS char* lptr;
#define GAS __attribute__((address_space(1)))
typedef GAS char* gptr;
typedef u64 v8q __attribute__((ext_vector_type(8)));

#define L_ 2048
#define D_ 1024
#define F_ 3584

#define OFF_E 0
#define OFF_F 0x4000
#define FREP 4
#define OFF_F2 (OFF_F + FREP * 2048)
#define OFF_G 0x2000
#define OFF_P 0x10000
#define OFF_IN 0x50000
#define OFF_H3 OFF_IN
#define OFF_H2 (OFF_IN + 0x70000)
#define OFF_KT (OFF_IN + 0xe0000)
#define KTP 3072
#define WS_USED (OFF_KT + 512 * KTP)
#define OFF_STAMP 0x1000000
#define SC1 16
#define NT 2
#define SPIN_CAP 20000
#define LSPIN_CAP 400000

#define FL_OFF 0
#define PR_OFF 256
#define SKIP_OFF 512
#define DUMP_OFF 1024
#define ROWB 528
#define KROWS 11648
#define F_TAGV 28
#define F_TAG 29
#define F_PUB 30
#define W_OFF 1536
#define S_OFF (W_OFF + 4 * KROWS)
#define U_OFF (S_OFF + 512)
#define UP 4672
#define T_OFF (S_OFF + 8 * UP)
#define T_P2 2304
#define T_P3 3584
#define TP 4352
#define G_OFF (T_OFF + 8 * TP)
#define MSK_OFF (G_OFF + 8192)
#define F_UG 8
#define F_GT 16
#define F_SKIP 24
#define F_CLR 25
#define GU_OFF W_OFF
#define XS_OFF (GU_OFF + 131072 + 2 * KROWS)
#define HS_OFF (XS_OFF + 4096)
#define KEY_OFF (HS_OFF + 8 * 272)
#define LDS_BYTES (KEY_OFF + 256)
static_assert(LDS_BYTES >= MSK_OFF + 5120 && LDS_BYTES <= 163840, "");
#define F_XS 1
#define F_KG 16
#define F_GU 2
#define F_FR 4
#define F_H 8

#define STAMP(k) do { } while (0)
#define STAMPC(k) do { } while (0)
#define STAMP_OUT() do { } while (0)

#define WIN 12
#define PSLEEP 1
#define CODE_LINES 198
#define RTIME 300
#define NCLR 4
#define WS_BASE 0
#define PFIRST 1
#define PREW 1
#define RAMP 3
#define CWIN 6
#define KTIME 20
#define CTIME 320
#define GTIME 370
#define FTIME 470
#define XDELAY 18
#define NPULL 0
#define CPULL 0
#define PTIME 8
#define WDELAY 0
#define H2DELAY 4
#define DWIN 16
#define EWIN 4
#define DEARLY 8
#define DAT 30
#define SAMEX 0
#define HTIME 600
#define USHIFT 2
#define XLD(T, p) __builtin_nontemporal_load((const T*)(p))
#define BARRIER() asm volatile("s_waitcnt lgkmcnt(0)\n\ts_barrier" ::: "memory")
#define END() return
#define CFENCE() asm volatile("" ::: "memory")
#define SCHED() __builtin_amdgcn_sched_barrier(0)

__device__ __forceinline__ int lane_id() {
    int l;
    asm volatile("v_mbcnt_lo_u32_b32 %0, -1, 0\n\tv_mbcnt_hi_u32_b32 %0, -1, %0" : "=v"(l));
    __builtin_assume(l >= 0 && l < 64);
    return l;
}
__device__ __forceinline__ float bflo(u32 w) { return __uint_as_float(w << 16); }
__device__ __forceinline__ float bfhi(u32 w) { return __uint_as_float(w & 0xffff0000u); }
__device__ __forceinline__ u32 pk(float lo, float hi) {
    f32x2 v = {lo, hi};
    return __builtin_bit_cast(u32, __builtin_convertvector(v, bf2_t));
}
__device__ __forceinline__ float rbf(float f) { return bflo(pk(f, f)); }
__device__ __forceinline__ f32x4 mfma16(v4u a, v4u b, f32x4 c) {
    return __builtin_amdgcn_mfma_f32_16x16x32_bf16(__builtin_bit_cast(bf16x8, a), __builtin_bit_cast(bf16x8, b), c, 0, 0, 0);
}
__device__ __forceinline__ v2u ldtr(lptr p) {
    return __builtin_bit_cast(v2u, __builtin_amdgcn_ds_read_tr16_b64_v4i16((LAS s16x4*)p));
}
template <typename T> __device__ __forceinline__ T lld(lptr p) { return *(LAS T*)p; }
template <typename T> __device__ __forceinline__ void lst(lptr p, T v) { *(LAS T*)p = v; }
__device__ __forceinline__ float sigm(float s) { return __builtin_amdgcn_rcpf(1.f + __expf(-s)); }

__device__ __forceinline__ u32 umax(u32 a, u32 b) { return a > b ? a : b; }
__device__ __forceinline__ u32 umin(u32 a, u32 b) { return a < b ? a : b; }
__device__ __forceinline__ u32 wave_max(u32 v) {
    v = umax(v, (u32)__builtin_amdgcn_update_dpp(0, (int)v, 0x111, 0xf, 0xf, false));
    v = umax(v, (u32)__builtin_amdgcn_update_dpp(0, (int)v, 0x112, 0xf, 0xf, false));
    v = umax(v, (u32)__builtin_amdgcn_update_dpp(0, (int)v, 0x114, 0xf, 0xf, false));
    v = umax(v, (u32)__builtin_amdgcn_update_dpp(0, (int)v, 0x118, 0xf, 0xf, false));
    v = umax(v, (u32)__builtin_amdgcn_update_dpp(0, (int)v, 0x142, 0xa, 0xf, false));
    v = umax(v, (u32)__builtin_amdgcn_update_dpp(0, (int)v, 0x143, 0xc, 0xf, false));
    return (u32)__builtin_amdgcn_readlane((int)v, 63);
}

#define TAGOK(q) (((q).x == tag) & ((q).z == tag))
#define WAITN(n) asm volatile("s_waitcnt vmcnt(%0)" :: "i"(n) : "memory")
#define WAITV(n) asm volatile("s_waitcnt vmcnt(" #n ")" ::: "memory")

#define BRKS 5,0,100,141,44,65
constexpr int is_brk(int n) { const int l[] = {BRKS}; for (int v : l) if (v == n) return 1; return 0; }
#define PAD_TXT(n, b) ".if %c" #b "\n\ts_branch GMX_PAD%c" #n "\n\t.p2align 12\n\t.endif\nGMX_PAD%c" #n ":\n\ts_cbranch_execz GMX_RET"
#define PAD(n) asm volatile(PAD_TXT(0, 1) :: "i"(n), "i"(is_brk(n)))
#define PADV(n, v) asm volatile(PAD_TXT(1, 2) : "+v"(v) : "i"(n), "i"(is_brk(n)))
#define COLD_BEGIN() asm volatile("GMX_COLDB:" ::: "memory")
#define COLD_END() asm volatile("GMX_COLDE:" ::: "memory")
#define NPT 32
#define NTS 5
#define NTC 6
#define PADTAB "GMX_PAD58,GMX_PAD69,GMX_PAD65,GMX_PAD66,GMX_PAD42,GMX_PAD43,GMX_PAD44,GMX_PAD87,GMX_PAD85,GMX_RET,GMX_RET,GMX_RET,GMX_RET,GMX_RET,GMX_RET,GMX_RET,GMX_PAD30,GMX_PAD100,GMX_PAD141,GMX_RET,GMX_RET,GMX_RET,GMX_RET,GMX_RET,GMX_RET,GMX_RET,GMX_RET,GMX_RET,GMX_RET,GMX_RET,GMX_RET,GMX_RET"
#define TOUCH_HEAD                                   \
    "s_mov_b64 s[84:85], exec\n\t"                   \
    "s_getpc_b64 s[80:81]\n"                         \
    "GMX_L%=:\n\t"                                   \
    "s_lshl_b32 s82, %0, 2\n\t"                      \
    "s_add_i32 s82, s82, GMX_TAB-GMX_L%=\n\t"        \
    "s_ashr_i32 s83, s82, 31\n\t"                    \
    "s_add_u32 s80, s80, s82\n\t"                    \
    "s_addc_u32 s81, s81, s83\n\t"                   \
    "s_mov_b64 exec, 0\n\t"                          \
    "s_swappc_b64 s[82:83], s[80:81]\n\t"            \
    "s_mov_b64 exec, s[84:85]\n\t"
#define TOUCH_CLOB "s80", "s81", "s82", "s83", "s84", "s85", "scc"
#define TOUCH(idx) asm volatile(TOUCH_HEAD :: "s"(idx) : TOUCH_CLOB)
#define TOUCH_TAB(idx) asm volatile(TOUCH_HEAD "s_branch GMX_END\nGMX_TAB:\n\t.irp t," PADTAB "\n\ts_branch \\t\n\t.endr\nGMX_END:" :: "s"(idx) : TOUCH_CLOB)
#define EARLY0 "GMX_PAD5"
#define EARLY1 "GMX_PAD0"
#define TOUCH_EARLY(sel) asm volatile("s_getpc_b64 s[82:83]\nGMX_E0:\n\ts_add_u32 s82, s82, GMX_E1-GMX_E0\n\ts_addc_u32 s83, s83, 0\n\ts_mov_b64 exec, 0\n\t"      \
    "s_bitcmp1_b32 %0, 0\n\ts_cbranch_scc1 " EARLY1 "\n\ts_branch " EARLY0 "\nGMX_RET:\n\ts_setpc_b64 s[82:83]\nGMX_E1:\n\ts_mov_b64 exec, -1" :: "s"(sel) : TOUCH_CLOB)
template <int N, int STR>
__device__ __forceinline__ void request(v4u (&q)[N], __amdgpu_buffer_rsrc_t rs, int off) {
#pragma unroll
    for (int k = 0; k < N; k++) q[k] = __builtin_amdgcn_raw_buffer_load_b128(rs, off, k * STR, SC1);
}
__device__ __forceinline__ u32 umax3(u32 a, u32 b, u32 c) { u32 r; asm("v_max3_u32 %0, %1, %2, %3" : "=v"(r) : "v"(a), "v"(b), "v"(c)); return r; }
__device__ __forceinline__ u32 umed3(u32 a, u32 b, u32 c) { u32 r; asm("v_med3_u32 %0, %1, %2, %3" : "=v"(r) : "v"(a), "v"(b), "v"(c)); return r; }
template <int N, bool TAGGED>
__device__ __forceinline__ bool arrived(const v4u (&q)[N], u32 tag) {
    u32 aa = ~0u, oo = 0u;
#pragma unroll
    for (int k = 0; k < N; k++) {
        if (TAGGED) { aa &= q[k].x & q[k].z; oo |= q[k].x | q[k].z; }
        else oo = umax3(umax3(oo, q[k].x, q[k].y), q[k].z, q[k].w);
    }
    return TAGGED ? (aa == tag) & (oo == tag) : oo != ~0u;
}
template <int N, int STR, bool TAGGED, int PID>
__device__ __forceinline__ void fetch(v4u (&q)[N], __amdgpu_buffer_rsrc_t rs, int off, u32 tag) {
    bool ok = false;
#pragma unroll 1
    for (int spins = 0; spins < SPIN_CAP; ++spins) {
        PAD(PID);
        CFENCE();
        if (!ok) request<N, STR>(q, rs, off);
        ok = arrived<N, TAGGED>(q, tag);
        if (__all(ok)) break;
        if (PSLEEP) __builtin_amdgcn_s_sleep(PSLEEP);
    }
}
__device__ __forceinline__ u32 pkd(float lo, float hi) { return umin(pk(lo, hi), 0xfffffffeu); }
#define DPP(v, ctl) ((u32)__builtin_amdgcn_update_dpp(0, (int)(v), ctl, 0xf, 0xf, true))
template <bool FIRST>
__device__ __forceinline__ void top8(u32& a, u32& b, int lane) {
    u32 t;
    const u32 c1 = (lane & 1) ? 0u : ~0u, c2 = (lane & 2) ? 0u : ~0u;
#define FLIP(ctl, c) t = umed3(a, DPP(b, ctl), c); b = umed3(b, DPP(a, ctl), c); a = t
#define HALF(ctl, c) t = umed3(a, DPP(a, ctl), c); b = umed3(b, DPP(b, ctl), c); a = t
#define INTRA() t = umax(a, b); b = umin(a, b); a = t
#define RESORT() HALF(0x4e, c2); HALF(0xb1, c1); INTRA()
    if (FIRST) {
        INTRA();
        FLIP(0xb1, c1); INTRA();
        FLIP(0x1b, c2); HALF(0xb1, c1); INTRA();
    }
    t = umax(a, DPP(b, 0x141)); b = umax(b, DPP(a, 0x141)); a = t; RESORT();
    t = umax(a, DPP(b, 0x140)); b = umax(b, DPP(a, 0x140)); a = t; RESORT();
    {
        const auto sa = __builtin_amdgcn_permlane16_swap(a, a, false, false), sb = __builtin_amdgcn_permlane16_swap(b, b, false, false);
        a = umax(sa[0], DPP(sb[1], 0x1b)); b = umax(sb[0], DPP(sa[1], 0x1b)); RESORT();
    }
    if (FIRST) {
        const auto sa = __builtin_amdgcn_permlane32_swap(a, a, false, false), sb = __builtin_amdgcn_permlane32_swap(b, b, false, false);
        a = umax(sa[0], DPP(sb[1], 0x1b)); b = umax(sb[0], DPP(sa[1], 0x1b)); RESORT();
    }
}
template <int PID>
__device__ __forceinline__ u32 dots(__amdgpu_buffer_rsrc_t rs, int off, u32 tag) {
    v4u q[4];
    fetch<4, 1024, true, PID>(q, rs, off, tag);
    const f32x2 lo = ((f32x2){bflo(q[0].y), bfhi(q[0].y)} + (f32x2){bflo(q[1].y), bfhi(q[1].y)}) + ((f32x2){bflo(q[2].y), bfhi(q[2].y)} + (f32x2){bflo(q[3].y), bfhi(q[3].y)});
    const f32x2 hi = ((f32x2){bflo(q[0].w), bfhi(q[0].w)} + (f32x2){bflo(q[1].w), bfhi(q[1].w)}) + ((f32x2){bflo(q[2].w), bfhi(q[2].w)} + (f32x2){bflo(q[3].w), bfhi(q[3].w)});
    const auto s0 = __builtin_amdgcn_permlane32_swap(__float_as_uint(lo[0]), __float_as_uint(hi[0]), false, false), s1 = __builtin_amdgcn_permlane32_swap(__float_as_uint(lo[1]), __float_as_uint(hi[1]), false, false);
    return pk(__uint_as_float(s0[0]) + __uint_as_float(s0[1]), __uint_as_float(s1[0]) + __uint_as_float(s1[1]));
}
struct c2 { f32x2 r, i; };
__device__ __forceinline__ c2 cmul(c2 a, c2 b) { return c2{a.r * b.r - a.i * b.i, a.r * b.i + a.i * b.r}; }
__device__ __forceinline__ c2 csq(c2 a) { const f32x2 t = a.r * a.i; return c2{a.r * a.r - a.i * a.i, t + t}; }
__device__ __forceinline__ c2 cpow(f32x2 l2, f32x2 rev) {
    c2 z;
#pragma unroll
    for (int e = 0; e < 2; e++) {
        const float m = __builtin_amdgcn_exp2f(l2[e]);
        const float f = __builtin_amdgcn_fractf(rev[e]);
        z.r[e] = m * __builtin_amdgcn_cosf(f);
        z.i[e] = m * __builtin_amdgcn_sinf(f);
    }
    return z;
}
typedef _Float16 h2_t __attribute__((ext_vector_type(2)));
typedef _Float16 h8_t __attribute__((ext_vector_type(8)));
__device__ __forceinline__ u32 pkh(float lo, float hi) { f32x2 v = {lo, hi}; return __builtin_bit_cast(u32, __builtin_convertvector(v, h2_t)); }
__device__ __forceinline__ v4u cmulh4(v4u a, v4u b) {
    u32 r0, r1, r2, r3;
    asm("v_pk_mul_f16 %0, %4, %8 op_sel_hi:[0,1]\n\tv_pk_mul_f16 %1, %5, %9 op_sel_hi:[0,1]\n\t"
        "v_pk_mul_f16 %2, %6, %10 op_sel_hi:[0,1]\n\tv_pk_mul_f16 %3, %7, %11 op_sel_hi:[0,1]\n\t"
        "v_pk_fma_f16 %0, %4, %8, %0 op_sel:[1,1,0] op_sel_hi:[1,0,1] neg_lo:[0,1,0]\n\tv_pk_fma_f16 %1, %5, %9, %1 op_sel:[1,1,0] op_sel_hi:[1,0,1] neg_lo:[0,1,0]\n\t"
        "v_pk_fma_f16 %2, %6, %10, %2 op_sel:[1,1,0] op_sel_hi:[1,0,1] neg_lo:[0,1,0]\n\tv_pk_fma_f16 %3, %7, %11, %3 op_sel:[1,1,0] op_sel_hi:[1,0,1] neg_lo:[0,1,0]"
        : "=&v"(r0), "=&v"(r1), "=&v"(r2), "=&v"(r3) : "v"(a[0]), "v"(a[1]), "v"(a[2]), "v"(a[3]), "v"(b[0]), "v"(b[1]), "v"(b[2]), "v"(b[3]));
    return (v4u){r0, r1, r2, r3};
}
__device__ __forceinline__ v2u cpk(c2 a) { return (v2u){pkh(a.r[0], a.i[0]), pkh(a.r[1], a.i[1])}; }
__device__ __forceinline__ v4u group_mask(int m, int lane) {
    const u32 lo = (0x34563u >> (4 * m)) & 15u, w = (0x18642u >> (4 * m)) & 15u, one = m < 4 ? 0x3f80u : 0xffffu;
    const u32 x = (u32)((lane & 15) + 16 - 8 * (lane >> 4)) - lo;
    v4u r;
#pragma unroll
    for (int d = 0; d < 4; d++) r[d] = (x - 2 * d <= w ? one : 0u) | (x - 2 * d - 1 <= w ? one << 16 : 0u);
    return r;
}
__device__ __forceinline__ void dma16cs(gptr sbase, u32 voff, u32 lds_dst) {
    asm volatile("s_mov_b32 m0, %2\n\ts_nop 0\n\tglobal_load_lds_dwordx4 %0, %1" :: "v"(voff), "s"(sbase), "s"(lds_dst) : "memory");
}
__device__ __forceinline__ void dma4s(gptr sbase, u32 voff, u32 lds_dst, bool nt) {
    if (nt) asm volatile("s_mov_b32 m0, %2\n\ts_nop 0\n\tglobal_load_lds_dword %0, %1 nt" :: "v"(voff), "s"(sbase), "s"(lds_dst) : "memory");
    else asm volatile("s_mov_b32 m0, %2\n\ts_nop 0\n\tglobal_load_lds_dword %0, %1" :: "v"(voff), "s"(sbase), "s"(lds_dst) : "memory");
}
template <int CTL> __device__ __forceinline__ float addd(float a, float b) {
    float r = __uint_as_float(DPP(__float_as_uint(a), CTL)) + b;
    asm("" : "+v"(r));
    return r;
}
#define SETF(f, v) do { CFENCE(); lst<u32>(lds + FL_OFF + (f) * 4, (u32)(v)); CFENCE(); } while (0)
__device__ __forceinline__ void waitf(lptr a, u32 v) {
#pragma unroll 1
    for (int spins = 0; spins < LSPIN_CAP; ++spins) {
        CFENCE();
        if (__all(lld<u32>(a) >= v)) break;
        __builtin_amdgcn_s_sleep(1);
    }
    CFENCE();
}
#define WAITF(f, v) waitf(lds + FL_OFF + (f) * 4, v)
__device__ __forceinline__ void swatch(gptr p, u32 tag) {
    asm volatile("s_movk_i32 s80, 100\n"
                 "GMX_W%=:\n\ts_load_dword s81, %0, 0 glc\n\ts_waitcnt lgkmcnt(0)\n\ts_cmp_eq_u32 s81, %1\n\ts_cbranch_scc1 GMX_X%=\n\t"
                 "s_addk_i32 s80, -1\n\ts_cmp_lg_u32 s80, 0\n\ts_cbranch_scc1 GMX_W%=\n"
                 "GMX_X%=:" :: "s"(p), "s"(tag) : "memory", "scc", "s80", "s81");
}
#define MKRS() __builtin_amdgcn_make_buffer_rsrc((void*)ws, 0, WS_USED, 0x00020000)
#define MKTAG(e) ((((e) + 1u) * 0x9E3779B1u ^ 0x5bd1e995u) & ~1u)
#define EPOCH() (*(const __attribute__((address_space(4))) u32*)(u64)(ws + OFF_E))

#define PADL(n) ".if %c[b" #n "]\ns_branch GMX_PAD" #n "\n.p2align 12\nGMX_PAD" #n ":\ns_cbranch_execz GMX_RET\n.elseif %c[all]\nGMX_PAD" #n ":\ns_cbranch_execz GMX_RET\n.else\ns_nop 0\n.endif\n"
#define LOOP_BRK(n) , [b##n] "i"(is_brk(n))
#define LOOP_BRKS , [all] "i"(is_brk(-1)) LOOP_BRK(100) LOOP_BRK(101) LOOP_BRK(102) LOOP_BRK(103) LOOP_BRK(104) LOOP_BRK(105) LOOP_BRK(106)
#define CLOB10(a) "v" #a "0", "v" #a "1", "v" #a "2", "v" #a "3", "v" #a "4", "v" #a "5", "v" #a "6", "v" #a "7", "v" #a "8", "v" #a "9"
#define LOOP_CLOB CLOB10(), CLOB10(1), CLOB10(2), CLOB10(3), CLOB10(4), CLOB10(5), CLOB10(6), CLOB10(7), CLOB10(8), CLOB10(9), CLOB10(10), CLOB10(11), \
    CLOB10(12), CLOB10(13), CLOB10(14), CLOB10(15), CLOB10(16), CLOB10(17), CLOB10(18), CLOB10(19), CLOB10(20), CLOB10(21), CLOB10(22), CLOB10(23), \
    "vcc", "scc", "s80", "memory"
#define CONV_LOOP \
    "ds_read_b128 v[188:191], %[ub] offset:448\n" "ds_read_b128 v[112:115], %[ma] offset:0\n" "ds_read_b128 v[116:119], %[ma] offset:1024\n" \
    "ds_read_b128 v[192:195], %[ub] offset:432\n" "ds_read_b128 v[120:123], %[ma] offset:2048\n" "ds_read_b128 v[124:127], %[ma] offset:3072\n" \
    "v_add_u32 v156, 0x400, %[pn]\n" "ds_read_b128 v[160:163], %[ub] offset:0\n" "ds_read2_b32 v[28:29], v156 offset0:32 offset1:34\n" \
    "ds_read2_b32 v[30:31], v156 offset0:36 offset1:38\n" "ds_read2_b32 v[60:61], %[pn] offset0:32 offset1:34\n" \
    "ds_read2_b32 v[62:63], %[pn] offset0:36 offset1:38\n" "ds_read_b128 v[164:167], %[ub] offset:64\n" \
    "ds_read2_b32 v[24:25], v156 offset0:64 offset1:66\n" "ds_read2_b32 v[26:27], v156 offset0:68 offset1:70\n" "s_waitcnt lgkmcnt(12)\n" \
    "v_mfma_f32_16x16x32_bf16 v[196:199], v[188:191], v[112:115], 0\n" "ds_read2_b32 v[56:57], %[pn] offset0:64 offset1:66\n" \
    "ds_read2_b32 v[58:59], %[pn] offset0:68 offset1:70\n" "s_waitcnt lgkmcnt(13)\n" "v_mfma_f32_16x16x32_bf16 v[200:203], v[188:191], v[116:119], 0\n" \
    "ds_read_b128 v[168:171], %[ub] offset:128\n" "ds_read2_b32 v[20:21], v156 offset0:96 offset1:98\n" \
    "ds_read2_b32 v[22:23], v156 offset0:100 offset1:102\n" "s_waitcnt lgkmcnt(15)\n" \
    "v_mfma_f32_16x16x32_bf16 v[204:207], v[192:195], v[112:115], 0\n" "ds_read2_b32 v[52:53], %[pn] offset0:96 offset1:98\n" \
    "ds_read2_b32 v[54:55], %[pn] offset0:100 offset1:102\n" "v_mfma_f32_16x16x32_bf16 v[208:211], v[192:195], v[116:119], 0\n" \
    "ds_read_b128 v[172:175], %[ub] offset:192\n" "ds_read2_b32 v[16:17], v156 offset0:128 offset1:130\n" \
    "ds_read2_b32 v[18:19], v156 offset0:132 offset1:134\n" "v_cvt_pk_bf16_f32 v212, v196, v197\n" "v_cvt_pk_bf16_f32 v213, v198, v199\n" \
    "s_waitcnt lgkmcnt(15)\n" "v_mfma_f32_16x16x32_bf16 v[196:199], v[188:191], v[120:123], 0\n" \
    "ds_read2_b32 v[48:49], %[pn] offset0:128 offset1:130\n" "ds_read2_b32 v[50:51], %[pn] offset0:132 offset1:134\n" \
    "v_cvt_pk_bf16_f32 v214, v200, v201\n" "v_cvt_pk_bf16_f32 v215, v202, v203\n" "v_mfma_f32_16x16x32_bf16 v[200:203], v[192:195], v[124:127], 0\n" \
    "ds_read_b128 v[176:179], %[ub] offset:256\n" "ds_read2_b32 v[12:13], v156 offset0:160 offset1:162\n" \
    "ds_read2_b32 v[14:15], v156 offset0:164 offset1:166\n" "v_mfma_f32_16x16x32_bf16 v[128:131], v[28:31], v[160:163], 0\n" \
    "ds_read2_b32 v[64:65], %[pn] offset0:0 offset1:2\n" "ds_read2_b32 v[66:67], %[pn] offset0:4 offset1:6\n" "s_waitcnt lgkmcnt(15)\n" \
    "v_mfma_f32_16x16x32_bf16 v[132:135], v[60:63], v[160:163], 0\n" "ds_read2_b32 v[44:45], %[pn] offset0:160 offset1:162\n" \
    "ds_read2_b32 v[46:47], %[pn] offset0:164 offset1:166\n" "v_mfma_f32_16x16x32_bf16 v[128:131], v[24:27], v[164:167], v[128:131]\n" \
    "ds_read_b128 v[180:183], %[ub] offset:320\n" "ds_read2_b32 v[8:9], v156 offset0:192 offset1:194\n" \
    "ds_read2_b32 v[10:11], v156 offset0:196 offset1:198\n" "v_mfma_f32_16x16x32_bf16 v[132:135], v[56:59], v[164:167], v[132:135]\n" \
    "ds_read2_b32 v[40:41], %[pn] offset0:192 offset1:194\n" "ds_read2_b32 v[42:43], %[pn] offset0:196 offset1:198\n" "s_waitcnt lgkmcnt(15)\n" \
    "v_mfma_f32_16x16x32_bf16 v[128:131], v[20:23], v[168:171], v[128:131]\n" "ds_read_b128 v[184:187], %[ub] offset:384\n" \
    "ds_read2_b32 v[4:5], v156 offset0:224 offset1:226\n" "ds_read2_b32 v[6:7], v156 offset0:228 offset1:230\n" \
    "v_mfma_f32_16x16x32_bf16 v[132:135], v[52:55], v[168:171], v[132:135]\n" "ds_read2_b32 v[36:37], %[pn] offset0:224 offset1:226\n" \
    "ds_read2_b32 v[38:39], %[pn] offset0:228 offset1:230\n" "v_add_u32 v157, 0x800, %[pn]\n" "s_waitcnt lgkmcnt(15)\n" \
    "v_mfma_f32_16x16x32_bf16 v[128:131], v[16:19], v[172:175], v[128:131]\n" "ds_read2_b32 v[0:1], v157 offset0:0 offset1:2\n" \
    "ds_read2_b32 v[2:3], v157 offset0:4 offset1:6\n" "v_mov_b32 v235, %[k1]\n" "ds_read_u16 v235, v235\n" \
    "v_mfma_f32_16x16x32_bf16 v[132:135], v[48:51], v[172:175], v[132:135]\n" "ds_read2_b32 v[32:33], v156 offset0:0 offset1:2\n" \
    "ds_read2_b32 v[34:35], v156 offset0:4 offset1:6\n" "ds_read_b128 v[108:111], %[ma] offset:4096\n" "s_waitcnt lgkmcnt(15)\n" \
    "v_mfma_f32_16x16x32_bf16 v[128:131], v[12:15], v[176:179], v[128:131]\n" "v_add_u32 v158, 0x280, %[p2]\n" "v_add_u32 v159, 0x500, %[p2]\n" \
    "v_mfma_f32_16x16x32_bf16 v[136:139], v[64:67], v[188:191], 0\n" "ds_read_b128 v[160:163], %[ub] offset:512\n" \
    "v_cvt_pk_bf16_f32 v216, v204, v205\n" "v_mfma_f32_16x16x32_bf16 v[132:135], v[44:47], v[176:179], v[132:135]\n" \
    "v_cvt_pk_bf16_f32 v217, v206, v207\n" "v_cvt_pk_bf16_f32 v218, v208, v209\n" "s_waitcnt lgkmcnt(14)\n" \
    "v_mfma_f32_16x16x32_bf16 v[128:131], v[8:11], v[180:183], v[128:131]\n" "ds_read_b128 v[164:167], %[ub] offset:576\n" \
    "v_cvt_pk_bf16_f32 v219, v210, v211\n" "s_waitcnt lgkmcnt(13)\n" "v_mfma_f32_16x16x32_bf16 v[132:135], v[40:43], v[180:183], v[132:135]\n" \
    "v_cvt_pk_bf16_f32 v220, v196, v197\n" "v_cvt_pk_bf16_f32 v221, v198, v199\n" "s_waitcnt lgkmcnt(10)\n" \
    "v_mfma_f32_16x16x32_bf16 v[128:131], v[4:7], v[184:187], v[128:131]\n" "ds_read_b32 v234, %[pr] offset:64\n" \
    "ds_read_b128 v[168:171], %[ub] offset:640\n" "s_waitcnt lgkmcnt(10)\n" "v_mfma_f32_16x16x32_bf16 v[132:135], v[36:39], v[184:187], v[132:135]\n" \
    "ds_read2_b32 v[68:69], %[p2] offset0:32 offset1:34\n" "ds_read2_b32 v[70:71], %[p2] offset0:112 offset1:114\n" "s_waitcnt lgkmcnt(10)\n" \
    "v_mfma_f32_16x16x32_bf16 v[128:131], v[0:3], v[188:191], v[128:131]\n" "s_waitcnt lgkmcnt(9)\n" "v_mul_u32_u24 v235, 0x10001, v235\n" \
    "s_waitcnt lgkmcnt(6)\n" "v_and_b32 v108, v108, v235\n" "v_mfma_f32_16x16x32_bf16 v[132:135], v[32:35], v[188:191], v[132:135]\n" \
    "v_and_b32 v109, v109, v235\n" "v_and_b32 v110, v110, v235\n" "v_and_b32 v111, v111, v235\n" "ds_read2_b32 v[72:73], %[p2] offset0:16 offset1:18\n" \
    "ds_read2_b32 v[74:75], %[p2] offset0:96 offset1:98\n" "v_mfma_f32_16x16x32_bf16 v[144:147], v[108:111], v[188:191], 0\n" \
    "ds_read2_b32 v[76:77], %[p2] offset0:0 offset1:2\n" "ds_read2_b32 v[78:79], %[p2] offset0:80 offset1:82\n" PADL(100) "s_waitcnt lgkmcnt(9)\n" \
    "v_mfma_f32_16x16x32_bf16 v[132:135], v[28:31], v[160:163], v[132:135]\n" "ds_read_b128 v[172:175], %[ub] offset:704\n" \
    "v_cvt_pk_bf16_f32 v222, v200, v201\n" "v_mfma_f32_16x16x32_bf16 v[136:139], v[60:63], v[160:163], v[136:139]\n" \
    "ds_read2_b32 v[80:81], v158 offset0:32 offset1:34\n" "ds_read2_b32 v[82:83], v158 offset0:112 offset1:114\n" "s_waitcnt lgkmcnt(11)\n" \
    "v_mfma_f32_16x16x32_bf16 v[132:135], v[24:27], v[164:167], v[132:135]\n" "v_cvt_pk_bf16_f32 v223, v202, v203\n" \
    "v_mfma_f32_16x16x32_bf16 v[136:139], v[56:59], v[164:167], v[136:139]\n" "ds_read2_b32 v[84:85], v158 offset0:16 offset1:18\n" \
    "ds_read2_b32 v[86:87], v158 offset0:96 offset1:98\n" "s_waitcnt lgkmcnt(11)\n" \
    "v_mfma_f32_16x16x32_bf16 v[132:135], v[20:23], v[168:171], v[132:135]\n" "v_cmp_gt_u32 vcc, 1, v234\n" "s_cbranch_vccnz GMX_Sg\n" "GMX_Rg:\n" \
    "ds_read2_b32 v[88:89], v158 offset0:0 offset1:2\n" "ds_read2_b32 v[90:91], v158 offset0:80 offset1:82\n" "s_waitcnt lgkmcnt(11)\n" \
    "v_mfma_f32_16x16x32_bf16 v[136:139], v[212:215], v[68:71], v[136:139]\n" "ds_read_b128 v[176:179], %[ub] offset:768\n" \
    "v_mfma_f32_16x16x32_bf16 v[136:139], v[52:55], v[168:171], v[136:139]\n" "ds_read2_b32 v[92:93], v159 offset0:48 offset1:50\n" \
    "ds_read2_b32 v[94:95], v159 offset0:144 offset1:146\n" "s_waitcnt lgkmcnt(12)\n" "v_mfma_f32_16x16x32_bf16 v[140:143], v[212:215], v[72:75], 0\n" \
    "s_waitcnt lgkmcnt(10)\n" "v_mfma_f32_16x16x32_bf16 v[144:147], v[212:215], v[76:79], v[144:147]\n" \
    "ds_read2_b32 v[96:97], v159 offset0:32 offset1:34\n" "ds_read2_b32 v[98:99], v159 offset0:128 offset1:130\n" "s_waitcnt lgkmcnt(11)\n" \
    "v_mfma_f32_16x16x32_bf16 v[132:135], v[16:19], v[172:175], v[132:135]\n" "ds_read2_b32 v[100:101], v159 offset0:16 offset1:18\n" \
    "ds_read2_b32 v[102:103], v159 offset0:112 offset1:114\n" "s_waitcnt lgkmcnt(11)\n" \
    "v_mfma_f32_16x16x32_bf16 v[136:139], v[216:219], v[80:83], v[136:139]\n" "ds_read_b128 v[180:183], %[ub] offset:832\n" \
    "v_mfma_f32_16x16x32_bf16 v[136:139], v[48:51], v[172:175], v[136:139]\n" "ds_read2_b32 v[104:105], v159 offset0:0 offset1:2\n" \
    "ds_read2_b32 v[106:107], v159 offset0:96 offset1:98\n" "s_waitcnt lgkmcnt(12)\n" \
    "v_mfma_f32_16x16x32_bf16 v[140:143], v[216:219], v[84:87], v[140:143]\n" "s_waitcnt lgkmcnt(10)\n" \
    "v_mfma_f32_16x16x32_bf16 v[144:147], v[216:219], v[88:91], v[144:147]\n" "ds_read_b128 v[188:191], %[ub] offset:960\n" "s_waitcnt lgkmcnt(10)\n" \
    "v_mfma_f32_16x16x32_bf16 v[132:135], v[12:15], v[176:179], v[132:135]\n" "s_waitcnt lgkmcnt(8)\n" \
    "v_mfma_f32_16x16x32_bf16 v[144:147], v[220:223], v[92:95], v[144:147]\n" "ds_read_b128 v[184:187], %[ub] offset:896\n" \
    "v_mfma_f32_16x16x32_bf16 v[136:139], v[44:47], v[176:179], v[136:139]\n" "ds_read_b128 v[192:195], %[ub] offset:944\n" "s_waitcnt lgkmcnt(8)\n" \
    "v_mfma_f32_16x16x32_bf16 v[148:151], v[220:223], v[96:99], 0\n" "s_waitcnt lgkmcnt(6)\n" \
    "v_mfma_f32_16x16x32_bf16 v[152:155], v[220:223], v[100:103], 0\n" "ds_read_b64 v[224:225], %[eb] offset:0\n" "s_waitcnt lgkmcnt(6)\n" \
    "v_mfma_f32_16x16x32_bf16 v[132:135], v[8:11], v[180:183], v[132:135]\n" "ds_read_b128 v[226:229], %[gb] offset:0\n" "s_waitcnt lgkmcnt(5)\n" \
    "v_mfma_f32_16x16x32_bf16 v[156:159], v[220:223], v[104:107], 0\n" "ds_read_b128 v[160:163], %[ub] offset:1024\n" \
    "v_mfma_f32_16x16x32_bf16 v[136:139], v[40:43], v[180:183], v[136:139]\n" "ds_read_b128 v[164:167], %[ub] offset:1088\n" "s_waitcnt lgkmcnt(6)\n" \
    "v_mfma_f32_16x16x32_bf16 v[204:207], v[188:191], v[112:115], 0\n" "ds_read_b128 v[168:171], %[ub] offset:1152\n" \
    "v_mfma_f32_16x16x32_bf16 v[208:211], v[188:191], v[116:119], 0\n" "ds_read_b128 v[172:175], %[ub] offset:1216\n" "s_waitcnt lgkmcnt(7)\n" \
    "v_mfma_f32_16x16x32_bf16 v[132:135], v[4:7], v[184:187], v[132:135]\n" "ds_read_b128 v[176:179], %[ub] offset:1280\n" "s_waitcnt lgkmcnt(7)\n" \
    "v_mfma_f32_16x16x32_bf16 v[196:199], v[192:195], v[112:115], 0\n" "v_cvt_pk_bf16_f32 v212, v204, v205\n" "v_cvt_pk_bf16_f32 v213, v206, v207\n" \
    "v_mfma_f32_16x16x32_bf16 v[136:139], v[36:39], v[184:187], v[136:139]\n" "v_cvt_pk_bf16_f32 v214, v208, v209\n" \
    "v_cvt_pk_bf16_f32 v215, v210, v211\n" "v_mfma_f32_16x16x32_bf16 v[200:203], v[192:195], v[116:119], 0\n" "s_waitcnt lgkmcnt(6)\n" \
    "v_lshlrev_b32 v230, 16, v224\n" "v_and_b32 v231, 0xffff0000, v224\n" "v_mfma_f32_16x16x32_bf16 v[204:207], v[188:191], v[120:123], 0\n" \
    "v_lshlrev_b32 v232, 16, v225\n" "v_and_b32 v233, 0xffff0000, v225\n" "v_mfma_f32_16x16x32_bf16 v[132:135], v[0:3], v[188:191], v[132:135]\n" \
    "v_fmac_f32 v128, %[dm], v230\n" "v_fmac_f32 v129, %[dm], v231\n" "v_mfma_f32_16x16x32_bf16 v[208:211], v[192:195], v[124:127], 0\n" \
    "v_fmac_f32 v130, %[dm], v232\n" "v_fmac_f32 v131, %[dm], v233\n" "v_mfma_f32_16x16x32_bf16 v[136:139], v[32:35], v[188:191], v[136:139]\n" \
    "s_waitcnt lgkmcnt(2)\n" "v_fmac_f32 v230, v226, v128\n" "v_fmac_f32 v231, v227, v129\n" \
    "v_mfma_f32_16x16x32_bf16 v[140:143], v[64:67], v[188:191], v[140:143]\n" "v_fmac_f32 v232, v228, v130\n" "v_fmac_f32 v233, v229, v131\n" \
    "v_mfma_f32_16x16x32_bf16 v[148:151], v[108:111], v[188:191], v[148:151]\n" PADL(101) \
    "v_mfma_f32_16x16x32_bf16 v[136:139], v[28:31], v[160:163], v[136:139]\n" "v_cvt_pk_bf16_f32 v128, v230, v231\n" \
    "v_cvt_pk_bf16_f32 v129, v232, v233\n" "v_mfma_f32_16x16x32_bf16 v[140:143], v[60:63], v[160:163], v[140:143]\n" \
    "ds_write_b64 %[sb], v[128:129] offset:0\n" "v_cvt_pk_bf16_f32 v216, v196, v197\n" \
    "v_mfma_f32_16x16x32_bf16 v[140:143], v[212:215], v[68:71], v[140:143]\n" "v_cvt_pk_bf16_f32 v217, v198, v199\n" \
    "v_cvt_pk_bf16_f32 v218, v200, v201\n" "v_mfma_f32_16x16x32_bf16 v[136:139], v[24:27], v[164:167], v[136:139]\n" \
    "ds_write_b32 %[pg], %[pg] offset:0\n" "v_cvt_pk_bf16_f32 v219, v202, v203\n" \
    "v_mfma_f32_16x16x32_bf16 v[144:147], v[212:215], v[72:75], v[144:147]\n" "ds_read_b64 v[224:225], %[eb] offset:512\n" \
    "v_cvt_pk_bf16_f32 v220, v204, v205\n" "v_mfma_f32_16x16x32_bf16 v[140:143], v[56:59], v[164:167], v[140:143]\n" \
    "ds_read_b128 v[226:229], %[gb] offset:1024\n" "v_cvt_pk_bf16_f32 v221, v206, v207\n" \
    "v_mfma_f32_16x16x32_bf16 v[148:151], v[212:215], v[76:79], v[148:151]\n" "ds_read_b128 v[188:191], %[ub] offset:1472\n" \
    "v_cvt_pk_bf16_f32 v222, v208, v209\n" "v_mfma_f32_16x16x32_bf16 v[136:139], v[20:23], v[168:171], v[136:139]\n" \
    "ds_read_b128 v[180:183], %[ub] offset:1344\n" "v_cvt_pk_bf16_f32 v223, v210, v211\n" \
    "v_mfma_f32_16x16x32_bf16 v[140:143], v[216:219], v[80:83], v[140:143]\n" "ds_read_b128 v[192:195], %[ub] offset:1456\n" \
    "v_mfma_f32_16x16x32_bf16 v[140:143], v[52:55], v[168:171], v[140:143]\n" "ds_read_b128 v[184:187], %[ub] offset:1408\n" \
    "v_mfma_f32_16x16x32_bf16 v[144:147], v[216:219], v[84:87], v[144:147]\n" "ds_read_b128 v[160:163], %[ub] offset:1536\n" "s_waitcnt lgkmcnt(8)\n" \
    "v_mfma_f32_16x16x32_bf16 v[136:139], v[16:19], v[172:175], v[136:139]\n" "ds_read_b128 v[164:167], %[ub] offset:1600\n" \
    "v_mfma_f32_16x16x32_bf16 v[148:151], v[216:219], v[88:91], v[148:151]\n" "ds_read_b128 v[168:171], %[ub] offset:1664\n" \
    "v_mfma_f32_16x16x32_bf16 v[140:143], v[48:51], v[172:175], v[140:143]\n" "s_waitcnt lgkmcnt(8)\n" "v_lshlrev_b32 v230, 16, v224\n" \
    "v_and_b32 v231, 0xffff0000, v224\n" "v_mfma_f32_16x16x32_bf16 v[148:151], v[220:223], v[92:95], v[148:151]\n" "v_lshlrev_b32 v232, 16, v225\n" \
    "v_and_b32 v233, 0xffff0000, v225\n" "v_mfma_f32_16x16x32_bf16 v[136:139], v[12:15], v[176:179], v[136:139]\n" "v_fmac_f32 v132, %[dm], v230\n" \
    "v_fmac_f32 v133, %[dm], v231\n" "v_mfma_f32_16x16x32_bf16 v[152:155], v[220:223], v[96:99], v[152:155]\n" "v_fmac_f32 v134, %[dm], v232\n" \
    "v_fmac_f32 v135, %[dm], v233\n" "v_mfma_f32_16x16x32_bf16 v[156:159], v[220:223], v[100:103], v[156:159]\n" "s_waitcnt lgkmcnt(4)\n" \
    "v_fmac_f32 v230, v226, v132\n" "v_fmac_f32 v231, v227, v133\n" "v_mfma_f32_16x16x32_bf16 v[140:143], v[44:47], v[176:179], v[140:143]\n" \
    "v_fmac_f32 v232, v228, v134\n" "v_fmac_f32 v233, v229, v135\n" "v_mfma_f32_16x16x32_bf16 v[196:199], v[188:191], v[112:115], 0\n" \
    "v_cvt_pk_bf16_f32 v132, v230, v231\n" "v_cvt_pk_bf16_f32 v133, v232, v233\n" \
    "v_mfma_f32_16x16x32_bf16 v[136:139], v[8:11], v[180:183], v[136:139]\n" "ds_write_b64 %[sb], v[132:133] offset:512\n" \
    "v_mfma_f32_16x16x32_bf16 v[200:203], v[188:191], v[116:119], 0\n" "v_mfma_f32_16x16x32_bf16 v[140:143], v[40:43], v[180:183], v[140:143]\n" \
    "ds_write_b32 %[pg], %[pg] offset:32\n" "v_mfma_f32_16x16x32_bf16 v[204:207], v[192:195], v[112:115], 0\n" "v_cvt_pk_bf16_f32 v212, v196, v197\n" \
    "v_cvt_pk_bf16_f32 v213, v198, v199\n" "s_waitcnt lgkmcnt(2)\n" "v_mfma_f32_16x16x32_bf16 v[136:139], v[4:7], v[184:187], v[136:139]\n" \
    "ds_read_b64 v[224:225], %[eb] offset:1024\n" "v_cvt_pk_bf16_f32 v214, v200, v201\n" \
    "v_mfma_f32_16x16x32_bf16 v[208:211], v[192:195], v[116:119], 0\n" "v_cvt_pk_bf16_f32 v215, v202, v203\n" \
    "ds_read_b128 v[226:229], %[gb] offset:2048\n" "v_mfma_f32_16x16x32_bf16 v[140:143], v[36:39], v[184:187], v[140:143]\n" \
    "ds_read_b128 v[172:175], %[ub] offset:1728\n" "v_cvt_pk_bf16_f32 v216, v204, v205\n" \
    "v_mfma_f32_16x16x32_bf16 v[196:199], v[188:191], v[120:123], 0\n" "ds_read_b128 v[176:179], %[ub] offset:1792\n" \
    "v_cvt_pk_bf16_f32 v217, v206, v207\n" "v_mfma_f32_16x16x32_bf16 v[136:139], v[0:3], v[188:191], v[136:139]\n" \
    "v_cvt_pk_bf16_f32 v218, v208, v209\n" "v_cvt_pk_bf16_f32 v219, v210, v211\n" "v_mfma_f32_16x16x32_bf16 v[200:203], v[192:195], v[124:127], 0\n" \
    "ds_read_b128 v[180:183], %[ub] offset:1856\n" "v_mfma_f32_16x16x32_bf16 v[140:143], v[32:35], v[188:191], v[140:143]\n" \
    "v_cvt_pk_bf16_f32 v220, v196, v197\n" "v_cvt_pk_bf16_f32 v221, v198, v199\n" \
    "v_mfma_f32_16x16x32_bf16 v[144:147], v[64:67], v[188:191], v[144:147]\n" "ds_read_b128 v[192:195], %[ub] offset:1968\n" \
    "v_mfma_f32_16x16x32_bf16 v[152:155], v[108:111], v[188:191], v[152:155]\n" PADL(102) \
    "v_mfma_f32_16x16x32_bf16 v[140:143], v[28:31], v[160:163], v[140:143]\n" "s_waitcnt lgkmcnt(5)\n" "v_lshlrev_b32 v230, 16, v224\n" \
    "v_and_b32 v231, 0xffff0000, v224\n" "v_mfma_f32_16x16x32_bf16 v[144:147], v[60:63], v[160:163], v[144:147]\n" "v_lshlrev_b32 v232, 16, v225\n" \
    "v_and_b32 v233, 0xffff0000, v225\n" "v_mfma_f32_16x16x32_bf16 v[144:147], v[212:215], v[68:71], v[144:147]\n" "v_fmac_f32 v136, %[dm], v230\n" \
    "v_fmac_f32 v137, %[dm], v231\n" "v_mfma_f32_16x16x32_bf16 v[140:143], v[24:27], v[164:167], v[140:143]\n" "v_fmac_f32 v138, %[dm], v232\n" \
    "v_fmac_f32 v139, %[dm], v233\n" "v_mfma_f32_16x16x32_bf16 v[148:151], v[212:215], v[72:75], v[148:151]\n" "s_waitcnt lgkmcnt(2)\n" \
    "v_fmac_f32 v230, v226, v136\n" "v_fmac_f32 v231, v227, v137\n" "v_mfma_f32_16x16x32_bf16 v[144:147], v[56:59], v[164:167], v[144:147]\n" \
    "v_fmac_f32 v232, v228, v138\n" "v_fmac_f32 v233, v229, v139\n" "v_mfma_f32_16x16x32_bf16 v[152:155], v[212:215], v[76:79], v[152:155]\n" \
    "ds_read_b128 v[188:191], %[ub] offset:1984\n" "v_cvt_pk_bf16_f32 v136, v230, v231\n" \
    "v_mfma_f32_16x16x32_bf16 v[140:143], v[20:23], v[168:171], v[140:143]\n" "v_cvt_pk_bf16_f32 v137, v232, v233\n" \
    "ds_write_b64 %[sb], v[136:137] offset:1024\n" "v_mfma_f32_16x16x32_bf16 v[144:147], v[216:219], v[80:83], v[144:147]\n" \
    "v_cvt_pk_bf16_f32 v222, v200, v201\n" "v_cvt_pk_bf16_f32 v223, v202, v203\n" \
    "v_mfma_f32_16x16x32_bf16 v[144:147], v[52:55], v[168:171], v[144:147]\n" "ds_write_b32 %[pg], %[pg] offset:64\n" \
    "v_mfma_f32_16x16x32_bf16 v[148:151], v[216:219], v[84:87], v[148:151]\n" "ds_read_b32 v234, %[pr] offset:32\n" \
    "v_mfma_f32_16x16x32_bf16 v[140:143], v[16:19], v[172:175], v[140:143]\n" "ds_read_b128 v[184:187], %[ub] offset:1920\n" \
    "v_mfma_f32_16x16x32_bf16 v[152:155], v[216:219], v[88:91], v[152:155]\n" "ds_read_b64 v[224:225], %[eb] offset:1536\n" \
    "v_mfma_f32_16x16x32_bf16 v[144:147], v[48:51], v[172:175], v[144:147]\n" "ds_read_b128 v[226:229], %[gb] offset:3072\n" \
    "v_mfma_f32_16x16x32_bf16 v[152:155], v[220:223], v[92:95], v[152:155]\n" "v_mfma_f32_16x16x32_bf16 v[140:143], v[12:15], v[176:179], v[140:143]\n" \
    "v_mfma_f32_16x16x32_bf16 v[156:159], v[220:223], v[96:99], v[156:159]\n" "v_mfma_f32_16x16x32_bf16 v[144:147], v[44:47], v[176:179], v[144:147]\n" \
    "s_waitcnt lgkmcnt(5)\n" "v_mfma_f32_16x16x32_bf16 v[204:207], v[188:191], v[112:115], 0\n" \
    "v_mfma_f32_16x16x32_bf16 v[140:143], v[8:11], v[180:183], v[140:143]\n" "s_waitcnt lgkmcnt(3)\n" "v_cmp_gt_u32 vcc, 2, v234\n" \
    "s_cbranch_vccnz GMX_Su\n" "GMX_Ru:\n" "v_mfma_f32_16x16x32_bf16 v[208:211], v[188:191], v[116:119], 0\n" \
    "ds_read_b128 v[160:163], %[ub] offset:2048\n" "v_mfma_f32_16x16x32_bf16 v[144:147], v[40:43], v[180:183], v[144:147]\n" \
    "ds_read_b128 v[164:167], %[ub] offset:2112\n" "v_cvt_pk_bf16_f32 v212, v204, v205\n" \
    "v_mfma_f32_16x16x32_bf16 v[196:199], v[192:195], v[112:115], 0\n" "v_cvt_pk_bf16_f32 v213, v206, v207\n" \
    "ds_read_b128 v[168:171], %[ub] offset:2176\n" "s_waitcnt lgkmcnt(3)\n" "v_mfma_f32_16x16x32_bf16 v[140:143], v[4:7], v[184:187], v[140:143]\n" \
    "v_cvt_pk_bf16_f32 v214, v208, v209\n" "v_cvt_pk_bf16_f32 v215, v210, v211\n" "v_mfma_f32_16x16x32_bf16 v[200:203], v[192:195], v[116:119], 0\n" \
    "ds_read_b128 v[172:175], %[ub] offset:2240\n" "v_lshlrev_b32 v230, 16, v224\n" \
    "v_mfma_f32_16x16x32_bf16 v[144:147], v[36:39], v[184:187], v[144:147]\n" "ds_read_b128 v[176:179], %[ub] offset:2304\n" \
    "v_and_b32 v231, 0xffff0000, v224\n" "v_mfma_f32_16x16x32_bf16 v[204:207], v[188:191], v[120:123], 0\n" "v_lshlrev_b32 v232, 16, v225\n" \
    "v_and_b32 v233, 0xffff0000, v225\n" "v_mfma_f32_16x16x32_bf16 v[140:143], v[0:3], v[188:191], v[140:143]\n" \
    "ds_read_b128 v[180:183], %[ub] offset:2368\n" "v_cvt_pk_bf16_f32 v216, v196, v197\n" \
    "v_mfma_f32_16x16x32_bf16 v[208:211], v[192:195], v[124:127], 0\n" "v_cvt_pk_bf16_f32 v217, v198, v199\n" "v_cvt_pk_bf16_f32 v218, v200, v201\n" \
    "v_mfma_f32_16x16x32_bf16 v[144:147], v[32:35], v[188:191], v[144:147]\n" "v_cvt_pk_bf16_f32 v219, v202, v203\n" \
    "ds_read_b128 v[184:187], %[ub] offset:2432\n" "v_mfma_f32_16x16x32_bf16 v[148:151], v[64:67], v[188:191], v[148:151]\n" \
    "v_fmac_f32 v140, %[dm], v230\n" "v_fmac_f32 v141, %[dm], v231\n" "v_mfma_f32_16x16x32_bf16 v[156:159], v[108:111], v[188:191], v[156:159]\n" \
    PADL(103) "s_waitcnt lgkmcnt(4)\n" "v_mfma_f32_16x16x32_bf16 v[144:147], v[28:31], v[160:163], v[144:147]\n" "v_fmac_f32 v142, %[dm], v232\n" \
    "v_fmac_f32 v143, %[dm], v233\n" "v_mfma_f32_16x16x32_bf16 v[148:151], v[60:63], v[160:163], v[148:151]\n" "v_fmac_f32 v230, v226, v140\n" \
    "v_fmac_f32 v231, v227, v141\n" "v_mfma_f32_16x16x32_bf16 v[144:147], v[24:27], v[164:167], v[144:147]\n" "v_fmac_f32 v232, v228, v142\n" \
    "v_fmac_f32 v233, v229, v143\n" "v_mfma_f32_16x16x32_bf16 v[148:151], v[212:215], v[68:71], v[148:151]\n" \
    "ds_read_b128 v[188:191], %[ub] offset:2496\n" "v_cvt_pk_bf16_f32 v140, v230, v231\n" \
    "v_mfma_f32_16x16x32_bf16 v[148:151], v[56:59], v[164:167], v[148:151]\n" "v_cvt_pk_bf16_f32 v141, v232, v233\n" \
    "ds_write_b64 %[sb], v[140:141] offset:1536\n" "v_mfma_f32_16x16x32_bf16 v[152:155], v[212:215], v[72:75], v[152:155]\n" \
    "v_cvt_pk_bf16_f32 v220, v204, v205\n" "v_cvt_pk_bf16_f32 v221, v206, v207\n" \
    "v_mfma_f32_16x16x32_bf16 v[144:147], v[20:23], v[168:171], v[144:147]\n" "ds_write_b32 %[pg], %[pg] offset:96\n" \
    "v_cvt_pk_bf16_f32 v222, v208, v209\n" "v_mfma_f32_16x16x32_bf16 v[156:159], v[212:215], v[76:79], v[156:159]\n" \
    "ds_read_b128 v[192:195], %[ub] offset:2480\n" "v_cvt_pk_bf16_f32 v223, v210, v211\n" \
    "v_mfma_f32_16x16x32_bf16 v[148:151], v[52:55], v[168:171], v[148:151]\n" "ds_read_b128 v[160:163], %[ub] offset:2560\n" \
    "v_mfma_f32_16x16x32_bf16 v[148:151], v[216:219], v[80:83], v[148:151]\n" "ds_read_b128 v[164:167], %[ub] offset:2624\n" "s_waitcnt lgkmcnt(6)\n" \
    "v_mfma_f32_16x16x32_bf16 v[144:147], v[16:19], v[172:175], v[144:147]\n" "ds_read_b128 v[168:171], %[ub] offset:2688\n" \
    "v_mfma_f32_16x16x32_bf16 v[148:151], v[48:51], v[172:175], v[148:151]\n" "ds_read_b64 v[224:225], %[eb] offset:2048\n" \
    "v_mfma_f32_16x16x32_bf16 v[152:155], v[216:219], v[84:87], v[152:155]\n" "ds_read_b128 v[172:175], %[ub] offset:2752\n" \
    "v_mfma_f32_16x16x32_bf16 v[144:147], v[12:15], v[176:179], v[144:147]\n" "ds_read_b128 v[226:229], %[gb] offset:4096\n" \
    "v_mfma_f32_16x16x32_bf16 v[156:159], v[216:219], v[88:91], v[156:159]\n" "v_mfma_f32_16x16x32_bf16 v[148:151], v[44:47], v[176:179], v[148:151]\n" \
    "ds_read_b128 v[176:179], %[ub] offset:2816\n" "v_mfma_f32_16x16x32_bf16 v[156:159], v[220:223], v[92:95], v[156:159]\n" \
    "v_mfma_f32_16x16x32_bf16 v[144:147], v[8:11], v[180:183], v[144:147]\n" "s_waitcnt lgkmcnt(6)\n" \
    "v_mfma_f32_16x16x32_bf16 v[196:199], v[188:191], v[112:115], 0\n" "v_mfma_f32_16x16x32_bf16 v[148:151], v[40:43], v[180:183], v[148:151]\n" \
    "ds_read_b128 v[180:183], %[ub] offset:2880\n" "v_mfma_f32_16x16x32_bf16 v[144:147], v[4:7], v[184:187], v[144:147]\n" "s_waitcnt lgkmcnt(4)\n" \
    "v_lshlrev_b32 v230, 16, v224\n" "v_and_b32 v231, 0xffff0000, v224\n" "v_mfma_f32_16x16x32_bf16 v[200:203], v[188:191], v[116:119], 0\n" \
    "v_lshlrev_b32 v232, 16, v225\n" "v_cvt_pk_bf16_f32 v212, v196, v197\n" "v_mfma_f32_16x16x32_bf16 v[148:151], v[36:39], v[184:187], v[148:151]\n" \
    "v_cvt_pk_bf16_f32 v213, v198, v199\n" "v_and_b32 v233, 0xffff0000, v225\n" "v_mfma_f32_16x16x32_bf16 v[204:207], v[192:195], v[112:115], 0\n" \
    "ds_read_b128 v[184:187], %[ub] offset:2944\n" "v_mfma_f32_16x16x32_bf16 v[144:147], v[0:3], v[188:191], v[144:147]\n" \
    "v_cvt_pk_bf16_f32 v214, v200, v201\n" "v_cvt_pk_bf16_f32 v215, v202, v203\n" "v_mfma_f32_16x16x32_bf16 v[208:211], v[192:195], v[116:119], 0\n" \
    "ds_read_b128 v[192:195], %[ub] offset:2992\n" "v_mfma_f32_16x16x32_bf16 v[148:151], v[32:35], v[188:191], v[148:151]\n" \
    "ds_read_b64 v[224:225], %[eb] offset:2560\n" "v_cvt_pk_bf16_f32 v216, v204, v205\n" \
    "v_mfma_f32_16x16x32_bf16 v[152:155], v[64:67], v[188:191], v[152:155]\n" PADL(104) \
    "v_mfma_f32_16x16x32_bf16 v[148:151], v[28:31], v[160:163], v[148:151]\n" "ds_read_b128 v[188:191], %[ub] offset:3008\n" \
    "v_cvt_pk_bf16_f32 v217, v206, v207\n" "v_mfma_f32_16x16x32_bf16 v[152:155], v[60:63], v[160:163], v[152:155]\n" \
    "v_cvt_pk_bf16_f32 v218, v208, v209\n" "v_cvt_pk_bf16_f32 v219, v210, v211\n" \
    "v_mfma_f32_16x16x32_bf16 v[148:151], v[24:27], v[164:167], v[148:151]\n" "v_fmac_f32 v144, %[dm], v230\n" "v_fmac_f32 v145, %[dm], v231\n" \
    "v_mfma_f32_16x16x32_bf16 v[152:155], v[212:215], v[68:71], v[152:155]\n" "v_fmac_f32 v146, %[dm], v232\n" "v_fmac_f32 v147, %[dm], v233\n" \
    "v_mfma_f32_16x16x32_bf16 v[152:155], v[56:59], v[164:167], v[152:155]\n" "s_waitcnt lgkmcnt(3)\n" "v_fmac_f32 v230, v226, v144\n" \
    "v_fmac_f32 v231, v227, v145\n" "v_mfma_f32_16x16x32_bf16 v[148:151], v[20:23], v[168:171], v[148:151]\n" "v_fmac_f32 v232, v228, v146\n" \
    "v_fmac_f32 v233, v229, v147\n" "v_mfma_f32_16x16x32_bf16 v[156:159], v[212:215], v[72:75], v[156:159]\n" "v_cvt_pk_bf16_f32 v144, v230, v231\n" \
    "v_cvt_pk_bf16_f32 v145, v232, v233\n" "v_mfma_f32_16x16x32_bf16 v[152:155], v[52:55], v[168:171], v[152:155]\n" \
    "ds_write_b64 %[sb], v[144:145] offset:2048\n" "s_waitcnt lgkmcnt(2)\n" "v_lshlrev_b32 v230, 16, v224\n" \
    "v_mfma_f32_16x16x32_bf16 v[152:155], v[216:219], v[80:83], v[152:155]\n" "v_and_b32 v231, 0xffff0000, v224\n" "v_lshlrev_b32 v232, 16, v225\n" \
    "v_mfma_f32_16x16x32_bf16 v[148:151], v[16:19], v[172:175], v[148:151]\n" "ds_write_b32 %[pg], %[pg] offset:128\n" \
    "v_and_b32 v233, 0xffff0000, v225\n" "v_mfma_f32_16x16x32_bf16 v[152:155], v[48:51], v[172:175], v[152:155]\n" \
    "ds_read_b128 v[160:163], %[ub] offset:3072\n" "v_mfma_f32_16x16x32_bf16 v[156:159], v[216:219], v[84:87], v[156:159]\n" \
    "ds_read_b128 v[164:167], %[ub] offset:3136\n" "v_mfma_f32_16x16x32_bf16 v[148:151], v[12:15], v[176:179], v[148:151]\n" \
    "ds_read_b128 v[168:171], %[ub] offset:3200\n" "v_mfma_f32_16x16x32_bf16 v[152:155], v[44:47], v[176:179], v[152:155]\n" \
    "ds_read_b128 v[172:175], %[ub] offset:3264\n" "s_waitcnt lgkmcnt(6)\n" "v_mfma_f32_16x16x32_bf16 v[196:199], v[188:191], v[112:115], 0\n" \
    "ds_read_b128 v[176:179], %[ub] offset:3328\n" "v_mfma_f32_16x16x32_bf16 v[148:151], v[8:11], v[180:183], v[148:151]\n" \
    "ds_read_b128 v[226:229], %[gb] offset:5120\n" "v_mfma_f32_16x16x32_bf16 v[200:203], v[188:191], v[116:119], 0\n" \
    "ds_read_b64 v[224:225], %[eb] offset:3072\n" "v_mfma_f32_16x16x32_bf16 v[152:155], v[40:43], v[180:183], v[152:155]\n" \
    "ds_read_b128 v[180:183], %[ub] offset:3392\n" "v_mfma_f32_16x16x32_bf16 v[148:151], v[4:7], v[184:187], v[148:151]\n" \
    "v_cvt_pk_bf16_f32 v212, v196, v197\n" "v_cvt_pk_bf16_f32 v213, v198, v199\n" "v_mfma_f32_16x16x32_bf16 v[204:207], v[192:195], v[112:115], 0\n" \
    "v_mfma_f32_16x16x32_bf16 v[152:155], v[36:39], v[184:187], v[152:155]\n" "ds_read_b128 v[184:187], %[ub] offset:3456\n" \
    "v_cvt_pk_bf16_f32 v214, v200, v201\n" "v_mfma_f32_16x16x32_bf16 v[148:151], v[0:3], v[188:191], v[148:151]\n" \
    "v_cvt_pk_bf16_f32 v215, v202, v203\n" "v_mfma_f32_16x16x32_bf16 v[208:211], v[192:195], v[116:119], 0\n" \
    "v_mfma_f32_16x16x32_bf16 v[152:155], v[32:35], v[188:191], v[152:155]\n" "v_mfma_f32_16x16x32_bf16 v[156:159], v[64:67], v[188:191], v[156:159]\n" \
    PADL(105) "s_waitcnt lgkmcnt(3)\n" "v_mfma_f32_16x16x32_bf16 v[152:155], v[28:31], v[160:163], v[152:155]\n" \
    "ds_read_b128 v[188:191], %[ub] offset:3520\n" "v_fmac_f32 v148, %[dm], v230\n" \
    "v_mfma_f32_16x16x32_bf16 v[156:159], v[60:63], v[160:163], v[156:159]\n" "ds_read_b128 v[160:163], %[ub] offset:3584\n" \
    "v_fmac_f32 v149, %[dm], v231\n" "v_mfma_f32_16x16x32_bf16 v[152:155], v[24:27], v[164:167], v[152:155]\n" "v_fmac_f32 v150, %[dm], v232\n" \
    "v_fmac_f32 v151, %[dm], v233\n" "v_mfma_f32_16x16x32_bf16 v[156:159], v[56:59], v[164:167], v[156:159]\n" "v_fmac_f32 v230, v226, v148\n" \
    "v_fmac_f32 v231, v227, v149\n" "v_mfma_f32_16x16x32_bf16 v[152:155], v[20:23], v[168:171], v[152:155]\n" "v_fmac_f32 v232, v228, v150\n" \
    "v_fmac_f32 v233, v229, v151\n" "v_mfma_f32_16x16x32_bf16 v[156:159], v[52:55], v[168:171], v[156:159]\n" \
    "ds_read_b128 v[164:167], %[ub] offset:3648\n" "v_cvt_pk_bf16_f32 v148, v230, v231\n" \
    "v_mfma_f32_16x16x32_bf16 v[152:155], v[16:19], v[172:175], v[152:155]\n" "v_cvt_pk_bf16_f32 v149, v232, v233\n" \
    "ds_write_b64 %[sb], v[148:149] offset:2560\n" "v_mfma_f32_16x16x32_bf16 v[156:159], v[48:51], v[172:175], v[156:159]\n" \
    "v_cvt_pk_bf16_f32 v216, v204, v205\n" "v_cvt_pk_bf16_f32 v217, v206, v207\n" \
    "v_mfma_f32_16x16x32_bf16 v[152:155], v[12:15], v[176:179], v[152:155]\n" "ds_write_b32 %[pg], %[pg] offset:160\n" \
    "v_cvt_pk_bf16_f32 v218, v208, v209\n" "v_mfma_f32_16x16x32_bf16 v[156:159], v[212:215], v[68:71], v[156:159]\n" \
    "ds_read_b128 v[168:171], %[ub] offset:3712\n" "v_cvt_pk_bf16_f32 v219, v210, v211\n" \
    "v_mfma_f32_16x16x32_bf16 v[156:159], v[44:47], v[176:179], v[156:159]\n" "ds_read_b128 v[172:175], %[ub] offset:3776\n" "s_waitcnt lgkmcnt(5)\n" \
    "v_lshlrev_b32 v230, 16, v224\n" "v_mfma_f32_16x16x32_bf16 v[152:155], v[8:11], v[180:183], v[152:155]\n" \
    "ds_read_b128 v[176:179], %[ub] offset:3840\n" "v_and_b32 v231, 0xffff0000, v224\n" \
    "v_mfma_f32_16x16x32_bf16 v[156:159], v[40:43], v[180:183], v[156:159]\n" "ds_read_b128 v[180:183], %[ub] offset:3904\n" \
    "v_lshlrev_b32 v232, 16, v225\n" "v_mfma_f32_16x16x32_bf16 v[152:155], v[4:7], v[184:187], v[152:155]\n" \
    "ds_read_b128 v[226:229], %[gb] offset:6144\n" "v_and_b32 v233, 0xffff0000, v225\n" \
    "v_mfma_f32_16x16x32_bf16 v[156:159], v[36:39], v[184:187], v[156:159]\n" "ds_read_b128 v[184:187], %[ub] offset:3968\n" \
    "v_mfma_f32_16x16x32_bf16 v[152:155], v[0:3], v[188:191], v[152:155]\n" "ds_read_b64 v[224:225], %[eb] offset:3584\n" \
    "v_mfma_f32_16x16x32_bf16 v[156:159], v[32:35], v[188:191], v[156:159]\n" "ds_read_b128 v[188:191], %[ub] offset:4032\n" \
    "v_mfma_f32_16x16x32_bf16 v[156:159], v[216:219], v[80:83], v[156:159]\n" PADL(106) \
    "v_mfma_f32_16x16x32_bf16 v[156:159], v[28:31], v[160:163], v[156:159]\n" "s_waitcnt lgkmcnt(7)\n" \
    "v_mfma_f32_16x16x32_bf16 v[156:159], v[24:27], v[164:167], v[156:159]\n" "v_fmac_f32 v152, %[dm], v230\n" "v_fmac_f32 v153, %[dm], v231\n" \
    "v_mfma_f32_16x16x32_bf16 v[156:159], v[20:23], v[168:171], v[156:159]\n" "v_fmac_f32 v154, %[dm], v232\n" "v_fmac_f32 v155, %[dm], v233\n" \
    "s_waitcnt lgkmcnt(5)\n" "v_mfma_f32_16x16x32_bf16 v[156:159], v[16:19], v[172:175], v[156:159]\n" \
    "v_mfma_f32_16x16x32_bf16 v[156:159], v[12:15], v[176:179], v[156:159]\n" "s_waitcnt lgkmcnt(3)\n" "v_fmac_f32 v230, v226, v152\n" \
    "v_fmac_f32 v231, v227, v153\n" "v_mfma_f32_16x16x32_bf16 v[156:159], v[8:11], v[180:183], v[156:159]\n" "v_fmac_f32 v232, v228, v154\n" \
    "v_fmac_f32 v233, v229, v155\n" "s_waitcnt lgkmcnt(2)\n" "v_mfma_f32_16x16x32_bf16 v[156:159], v[4:7], v[184:187], v[156:159]\n" \
    "v_cvt_pk_bf16_f32 v152, v230, v231\n" "v_cvt_pk_bf16_f32 v153, v232, v233\n" "s_waitcnt lgkmcnt(0)\n" \
    "v_mfma_f32_16x16x32_bf16 v[156:159], v[0:3], v[188:191], v[156:159]\n" "ds_write_b64 %[sb], v[152:153] offset:3072\n" \
    "v_lshlrev_b32 v230, 16, v224\n" "ds_write_b32 %[pg], %[pg] offset:192\n" "ds_read_b128 v[226:229], %[gb] offset:7168\n" \
    "v_and_b32 v231, 0xffff0000, v224\n" "v_lshlrev_b32 v232, 16, v225\n" "v_and_b32 v233, 0xffff0000, v225\n" "s_nop 0\n" \
    "v_fmac_f32 v156, %[dm], v230\n" "v_fmac_f32 v157, %[dm], v231\n" "v_fmac_f32 v158, %[dm], v232\n" "v_fmac_f32 v159, %[dm], v233\n" \
    "s_waitcnt lgkmcnt(0)\n" "v_fmac_f32 v230, v226, v156\n" "v_fmac_f32 v231, v227, v157\n" "v_fmac_f32 v232, v228, v158\n" \
    "v_fmac_f32 v233, v229, v159\n" "v_cvt_pk_bf16_f32 v156, v230, v231\n" "v_cvt_pk_bf16_f32 v157, v232, v233\n" \
    "ds_write_b64 %[sb], v[156:157] offset:3584\n" "ds_write_b32 %[pg], %[pg] offset:224\n" "s_branch GMX_LE\n" "GMX_Sg:\n" "s_mov_b32 s80, 0x40000\n" \
    "GMX_Tg:\n" "s_sleep 1\n" "ds_read_b32 v234, %[pr] offset:64\n" "s_waitcnt lgkmcnt(0)\n" "v_cmp_gt_u32 vcc, 1, v234\n" "s_cbranch_vccz GMX_Ug\n" \
    "s_sub_u32 s80, s80, 1\n" "s_cbranch_scc0 GMX_Tg\n" "GMX_Ug:\n" "s_branch GMX_Rg\n" "GMX_Su:\n" "s_mov_b32 s80, 0x40000\n" "GMX_Tu:\n" \
    "s_sleep 1\n" "ds_read_b32 v234, %[pr] offset:32\n" "s_waitcnt lgkmcnt(0)\n" "v_cmp_gt_u32 vcc, 2, v234\n" "s_cbranch_vccz GMX_Uu\n" \
    "s_sub_u32 s80, s80, 1\n" "s_cbranch_scc0 GMX_Tu\n" "GMX_Uu:\n" "s_branch GMX_Ru\n" "GMX_LE:\n"

__global__ void __launch_bounds__(512, 2) __attribute__((aligned(4096)))
fused_kernel(const void*, const void*, const void*, void*, const void*, const void*, const void*, const void*,
             const void*, const void*, const void*, const void*, const void*, void*, const void*, const void*) {
    __shared__ __attribute__((aligned(16))) char smem[LDS_BYTES];
    const lptr lds = (lptr)smem;
    const int tid = threadIdx.x, lane = tid & 63;
    v8q ka0, ka1;
    u64 tstart64;
    asm volatile("s_memrealtime %2\n\ts_load_dwordx16 %0, %3, 0\n\ts_load_dwordx16 %1, %3, 64" : "=&s"(ka0), "=&s"(ka1), "=&s"(tstart64) : "s"(__builtin_amdgcn_kernarg_segment_ptr()));
    SCHED();
    const int wave = __builtin_amdgcn_readfirstlane(tid >> 6);
    const u32 lds0 = (u32)(uintptr_t)lds;
    {
        const u32 ln = umin((blockIdx.x & ~7u) + wave, CODE_LINES - 1);
        dma4s((gptr)__builtin_amdgcn_s_getpc() + ln * 128, 0, lds0 + DUMP_OFF, false);
    }
    lst<v4u>(lds + FL_OFF + lane * 16, (v4u){0u, 0u, 0u, 0u});
    int role = (blockIdx.x >> 5) & 1;
    const int i = lane & 15, g = lane >> 4;
    const int xcd = blockIdx.x & 7, jx = ((blockIdx.x >> 3) & 3) | ((blockIdx.x >> 6) << 2);
    const int pb = xcd * 16 + jx;
    const int ct = SAMEX ? (xcd * 14 + jx) >> 2 : (xcd >> 2) * 14 + jx, kq = SAMEX ? (xcd * 14 + jx) & 3 : xcd & 3;
    const bool has_w = jx < 14;
    const int ctu = ct + USHIFT - (ct + USHIFT >= 28 ? 28 : 0);
    u32 vo = (u32)(xcd * 256 + (g & 1) * 128 + (i & 3) * 16), xo0 = (u32)(((blockIdx.x >> 3) * 64 + 16 * (i >> 2) + 8 * (g >> 1)) * 2048) + vo;
    u32 duty = (pb & 63) == 14 ? 0x01111111u : (pb & 63) == 30 ? 0x8aaaaaaau : 0x0111aaaau;
    duty = has_w ? 0x0133aaaau : duty;
    duty = (role ? 0x0411aaaau : duty) >> (4 * wave);
    int kind = duty & 7, kg = duty & 8;
    const int ch = 8 * pb + wave + (role ? 0 : wave < 4 ? 4 : -128);
    u32 po0 = (u32)(ch * 256 + lane * 4), so0 = (u32)((ch >> 1) * 4);
    u32 ld0 = lds0 + W_OFF + wave * KROWS + (role * 4 + wave < 4 ? 131072 - 2 * KROWS : 0);
    asm volatile("" : "+v"(xo0), "+v"(vo), "+v"(po0), "+s"(ld0), "+v"(so0), "+s"(kind), "+s"(role), "+s"(kg));
    if (kind & 1) TOUCH_EARLY(wave);
    SCHED();
    asm volatile("s_waitcnt lgkmcnt(0)" : "+s"(ka0), "+s"(ka1), "+s"(tstart64) :: "memory");
    const u32 tstart = (u32)tstart64;
    const gptr x = (gptr)ka0[0], wr = (gptr)ka0[1], gw = (gptr)ka0[2], ws = (gptr)ka0[3], wg = (gptr)ka0[4], wu = (gptr)ka0[5], logdt = (gptr)ka0[6], dskip = (gptr)ka0[7];
    const gptr logA = (gptr)ka1[0], Aim = (gptr)ka1[1], Cre = (gptr)ka1[2], Cim = (gptr)ka1[3], wd = (gptr)ka1[4], out = (gptr)ka1[5];
    SCHED();
#define PARAMS() asm volatile("s_mov_b32 m0, %2\n\ts_nop 0\n\tglobal_load_lds_dword %0, %3 nt\n\ts_addk_i32 m0, 0x100\n\ts_nop 0\n\tglobal_load_lds_dword %1, %7\n\t"            \
                     "s_addk_i32 m0, 0x100\n\ts_nop 0\n\tglobal_load_lds_dword %0, %4 nt\n\ts_addk_i32 m0, 0x100\n\ts_nop 0\n\tglobal_load_lds_dword %0, %5 nt\n\t" \
                     "s_addk_i32 m0, 0x100\n\ts_nop 0\n\tglobal_load_lds_dword %0, %6 nt\n\ts_addk_i32 m0, 0x100\n\ts_nop 0\n\tglobal_load_lds_dword %1, %8"       \
                     :: "v"(po0), "v"(so0), "s"(ld0), "s"(logA), "s"(Aim), "s"(Cre), "s"(Cim), "s"(logdt), "s"(dskip) : "memory", "scc")
    u32 tag = 0;
    const __amdgpu_buffer_rsrc_t rs = MKRS(), rx = __builtin_amdgcn_make_buffer_rsrc((void*)x, 0, L_ * D_ * 2, 0x00020000);
    if (kg) PARAMS();
    if (kind == 4) {
#pragma unroll 1
        for (int p = 0; p < 7; p++) __builtin_amdgcn_raw_buffer_store_b128((v4u){~0u, ~0u, ~0u, ~0u}, rs, OFF_IN + pb * 7168 + lane * 16 + p * 1024, 0, SC1);
    }
    if (__builtin_expect(kind == 0, 1)) {
        v4u xa[16], wb[2];
        {
            const gptr vp = ((i & 4) ? gw : wr) + vo;
            wb[0] = *(const GAS v4u*)vp; wb[1] = *(const GAS v4u*)(vp + 64);
        }
        SCHED();
#define XROW(p) xa[p] = ((p) & 1) ? __builtin_amdgcn_raw_buffer_load_b128(rx, xo0 + 64 + ((p) & 2) * 1024, ((p) >> 2) * 4096, 0) : __builtin_amdgcn_raw_buffer_load_b128(rx, xo0 + ((p) & 2) * 1024, ((p) >> 2) * 4096, NT)
#pragma unroll
        for (int p = 0; p < 16; p++) {
            XROW(p);
            SCHED();
        }
        tag = EPOCH();
        int ro = OFF_P + ((((i >> 2) & 1) * 16 + (blockIdx.x >> 4)) * 8 + xcd) * 512 + ((blockIdx.x >> 3) & 1) * 256 + g * 64 + (i >> 3) * 32 + (i & 3) * 8;
        if (PREW) {
            __builtin_amdgcn_raw_buffer_store_b64((v2u){~0u, ~0u}, rs, ro, 0, SC1);
        }
        asm volatile("s_barrier" ::: "memory");
        __builtin_amdgcn_s_setprio(3);
        tag = MKTAG(tag);
        asm volatile("" : "+s"(tag), "+v"(ro));
        lst<u32>(lds + FL_OFF + F_TAGV * 4, tag); SETF(F_TAG, 1);
        STAMP(13);
        SCHED();
        if ((i >> 3) != (g >> 1)) wb[0] = wb[1] = (v4u){0, 0, 0, 0};
        SCHED();
        float s[2] = {0.f, 0.f}, v[4];
        f32x4 ac[8];
        u32 clv;
#define QSUM(h) do { \
            _Pragma("unroll") for (int t = 0; t < 4; t++) v[t] = __uint_as_float(DPP(__float_as_uint(ac[4 * (h) + t][0]), 0x00)); \
            SCHED(); _Pragma("unroll") for (int t = 0; t < 4; t++) v[t] = addd<0x55>(ac[4 * (h) + t][1], v[t]); \
            SCHED(); _Pragma("unroll") for (int t = 0; t < 4; t++) v[t] = addd<0xaa>(ac[4 * (h) + t][2], v[t]); \
            SCHED(); _Pragma("unroll") for (int t = 0; t < 4; t++) v[t] = addd<0xff>(ac[4 * (h) + t][3], v[t]); \
            SCHED(); _Pragma("unroll") for (int t = 0; t < 4; t++) s[t & 1] = (i & 3) == 2 * (h) + (t >> 1) ? v[t] : s[t & 1]; \
            SCHED(); } while (0)
#pragma unroll
        for (int p = 0; p < 16; p++) {
            ac[p >> 1] = mfma16(xa[p], wb[p & 1], (p & 1) ? ac[p >> 1] : (f32x4){0, 0, 0, 0});
            SCHED();
            if (p == 0) STAMP(14);
            if (p == 9) QSUM(0);
            if (p == 13) { clv = lld<u32>(lds + FL_OFF + F_CLR * 4); SCHED(); }
        }
        STAMP(15);
        const u32 clr = __builtin_amdgcn_readfirstlane(clv);
        QSUM(1);
        const v2u o = {tag, pk(s[0], s[1])};
        if (role && !clr) WAITF(F_CLR, 1);
        __builtin_amdgcn_raw_buffer_store_b64(o, rs, ro, 0, SC1);
        SETF(F_PUB, 1);
        STAMP(10);
    }
    asm volatile("" : "+s"(kind));
    if (kind != 0) {
        STAMP(12);
        asm volatile("s_barrier" ::: "memory");
        if (kind == 3) {
            const int lw = wave & 1;
            const u32 ro = (u32)(8 * (lane >> 5) + ((lane >> 3) & 3)), co = (u32)(((lane & 7) ^ ((lane >> 3) & 6)) << 4);
            gptr wp = (lw ? wu + 256 * ctu : wg + 256 * ct) + (u32)(256 * kq * 7168);
            u32 pitch = 7168, ld = lds0 + GU_OFF + lw * 2048, cnt = 0;
            STAMP(13);
#define GU_REQ(v, note) "s_waitcnt vmcnt(%9)\n\tglobal_load_lds_dwordx4 " v ", s[80:81] nt\n\ts_addk_i32 m0, 0x380\n\ts_waitcnt vmcnt(%9)\n\t" note     \
                        "global_load_lds_dwordx4 " v ", s[80:81] offset:128 nt\n\ts_addk_i32 m0, 0xc80\n\t"
#define GU_NOTE(w) ".if %9 == " #w "\n\tds_write_b32 %8, %2\n\tv_add_u32 %2, 1, %2\n\t.endif\n\t"
            static_assert(WIN == 10 || WIN == 12 || WIN == 14 || WIN == 16, "");
#pragma unroll 1
            for (int ph = 0; ph < 3; ph++) {
                if (ph == 1) waitf(lds + FL_OFF + (F_KG + (lane & 1)) * 4, 1);
                if (ph == 2) {
                    STAMP(16);
                    waitf(lds + FL_OFF + (F_FR + 2 * lw + (lane & 1)) * 4, 1);
                    wp = wd + (u32)(128 * ct * 2048 + 512 * kq + 256 * lw); pitch = 2048; ld -= 131072;
                }
                const u32 wo = ro * pitch + co;
                asm volatile("s_mov_b32 m0, %0\n\ts_add_i32 %0, %0, %10\n\ts_mov_b64 s[80:81], %1\n"
                             "GMX_GU:\n\t" GU_REQ("%3", GU_NOTE(9)) GU_REQ("%4", GU_NOTE(11)) GU_REQ("%5", GU_NOTE(13)) GU_REQ("%6", GU_NOTE(15))
                             "s_add_u32 s80, s80, %7\n\ts_addc_u32 s81, s81, 0\n\ts_cmp_lg_u32 m0, %0\n\ts_cbranch_scc1 GMX_GU\n\ts_mov_b64 %1, s[80:81]"
                             : "+s"(ld), "+s"(wp), "+v"(cnt) : "v"(wo), "v"(wo + 16 * pitch), "v"(wo + 4 * pitch), "v"(wo + 20 * pitch), "s"(32 * pitch),
                               "v"(lds0 + FL_OFF + (F_GU + lw) * 4), "i"(WIN - 1), "s"(((0x426 >> (4 * ph)) & 15) * 16384) : "memory", "scc", "s80", "s81");
            }
            STAMP(17);
            WAITV(8); SETF(F_GU + lw, 12);
            WAITV(0); SETF(F_GU + lw, 13);
            STAMP(18);
            STAMP_OUT();
        }
        if (kind == 4) { WAITV(0); SETF(F_CLR, 1); }
        if (kind == 1) {
#pragma unroll 1
            for (int t = role ? NPT / 2 - 4 + wave : has_w ? 0 : wave % 3; t < (role ? NPT / 2 + NTC : has_w ? NPT / 2 : NTS); t += role ? 2 : has_w ? 1 : 3) TOUCH_TAB(t);
        }
    }
    asm volatile("" : "+s"(role), "+s"(kg));

    const lptr wl = (lptr)ld0;
    const int r32 = lane & 31, h = lane >> 5;
    u32 kp[6], dsk = 0;
    const lptr tl = lds + T_OFF + wave * TP;
    lptr tn, t2, t3, pn, p2, ub, eb, sb, gb, pg, ma, pr;
#define CONV_SETUP() do { \
        tn = tl + (288 - 8 * r32 - 4 * h) * 4; \
        t2 = tl + T_P2 + (2 * (r32 & 1) * 80 + 52 - 16 * h - (r32 >> 1)) * 4 - 2; \
        t3 = tl + T_P3 + ((r32 & 1) * 96 + 52 - 32 * h - (r32 >> 1)) * 4 - 2; \
        pn = tl + (20 - i + 8 * g) * 4; p2 = tl + T_P2 + (20 - i + 4 * g) * 4; \
        ub = lds + U_OFF + wave * UP + (16 * i + 8 * g - 16) * 2 - 448; \
        eb = lds + U_OFF + wave * UP + (16 * i + 4 * g) * 2; \
        sb = lds + S_OFF + wave * UP + 32 * i + 8 * (g ^ (i >> 2)); \
        gb = lds + G_OFF + (16 * i + 4 * g) * 4; \
        pg = lds + PR_OFF + wave * 4; ma = lds + MSK_OFF + lane * 16; pr = lds + FL_OFF + (lane & 7) * 4; \
        asm volatile("" : "+v"(tn), "+v"(t2), "+v"(t3), "+v"(pn), "+v"(p2), "+v"(ub), "+v"(eb), "+v"(sb), "+v"(gb), "+v"(pg), "+v"(ma), "+v"(pr)); \
        lst<v2u>(lds + S_OFF + wave * UP + lane * 8, (v2u){0, 0}); \
        lst<v4u>(tl + T_P2 + lane * 16, (v4u){0, 0, 0, 0}); lst<v4u>(tl + T_P2 + 1024 + lane * 16, (v4u){0, 0, 0, 0}); \
        lst<u32>(tl + (lane < 32 ? lane : lane + 512) * 4, 0u); \
    } while (0)
    if (role && kg) { PAD(1); CONV_SETUP(); }
    if (kg) {
        PAD(2);
        lptr ea_, pa, qa;
        {
            const u64 c1 = 0x0d0c0b080b0a0904ull, c2 = 0x0f0e151413121110ull;
            const int erow = (r32 < 8 || r32 >= 24) ? (r32 & 7) : (int)(((r32 < 16 ? c1 : c2) >> (8 * (r32 & 7))) & 31u);
            ea_ = wl + erow * ROWB + h * 16;
            pa = wl + (r32 >> 2) * ROWB + h * 16; qa = wl + ((r32 & 3) ? 7 + (r32 & 3) : 0) * ROWB + h * 16;
            asm volatile("" : "+v"(ea_), "+v"(pa), "+v"(qa));
        }
        PAD(4);
        __builtin_amdgcn_s_setprio(2);
        WAITV(0);
        STAMP(1);
        const u32 pla = lld<u32>(wl + lane * 4), sld = lld<u32>(wl + 256 + lane * 4), pai = lld<u32>(wl + 512 + lane * 4);
        const u32 pcr = lld<u32>(wl + 768 + lane * 4), pci = lld<u32>(wl + 1024 + lane * 4), sds = lld<u32>(wl + 1280 + lane * 4);
        const float ldt = (wave & 1) ? bfhi(sld) : bflo(sld);
        dsk = sds;
        {
            const float dt = rbf(__expf(ldt));
            const u32 ea = pk(__expf(bflo(pla)), __expf(bfhi(pla)));
            const f32x2 l2 = (f32x2){bflo(ea) * dt, bfhi(ea) * dt} * -1.4426950408889634f;
            const f32x2 rev = (f32x2){bflo(pai) * dt, bfhi(pai) * dt} * -0.15915494309189535f;
            const u32 cm = umax(pcr & 0x7f807f80u, pci & 0x7f807f80u), cl = umax((pcr << 16) & 0x7f800000u, (pci << 16) & 0x7f800000u);
            const u32 ebits = umin(wave_max(umax(cm, cl)) & 0x7f800000u, 0x7e800000u);
            const float cinv = __uint_as_float(0x7f000000u - ebits), kscale = 2.f * dt * __uint_as_float(ebits);
            const c2 z1 = cpow(l2, rev), zB = cpow(l2 * 256.f, rev * 256.f);
            const c2 z2 = csq(z1), z4 = csq(z2);
            PAD(5);
            const lptr wp = wl + lane * 8;
#define PUTP(row, pv) lst<v2u>(wp + (row) * ROWB, pv)
#define PUT(row, v) PUTP(row, cpk(v))
            const c2 e0 = c2{(f32x2){bflo(pcr), bfhi(pcr)} * cinv, (f32x2){bflo(pci), bfhi(pci)} * -cinv};
            const c2 e4 = cmul(e0, z4), e2 = cmul(e0, z2);
            PUT(0, e0); PUT(9, cmul(e0, zB));
            SCHED();
            c2 t8 = cmul(e4, z4), t4c = cmul(e4, zB), q1 = csq(z4), q2, p1, p2_, p3_, p4;
            q1.i = -q1.i;
            PUT(4, e4); PUT(2, e2);
            SCHED();
            const c2 e6 = cmul(e4, z2);
            PUT(8, t8); PUT(11, t4c);
            SCHED();
#pragma unroll
            for (int st = 0; st < 7; st++) {
                PADV(6 + st, t8.r);
                t8 = cmul(t8, zB); PUT(st == 0 ? 13 : st == 1 ? 17 : st == 2 ? 21 : st < 5 ? 15 + st : 9 + st, t8);
                if (st < 2) { t4c = cmul(t4c, zB); PUT(st == 0 ? 16 : 20, t4c); }
                if (st == 0) PUT(6, e6);
                if (st == 2) { PUT(10, cmul(e2, zB)); PUT(12, cmul(e6, zB)); }
                if (st >= 3) PUT(2 * (st - 3) + 1, cmul(st == 3 ? e0 : st == 4 ? e2 : st == 5 ? e4 : e6, z1));
                if (st == 3) q2 = csq(q1);
                if (st == 4) p1 = csq(q2);
                if (st == 5) p2_ = csq(p1);
                if (st == 6) p4 = csq(p2_);
                SCHED();
            }
            STAMP(2);
            PAD(13);
            v4u ef[16];
#pragma unroll
            for (int s = 0; s < 16; s++) ef[s] = lld<v4u>(ea_ + s * 32);
            SCHED();
            p3_ = cmul(p2_, p1);
            PUTP(0, ((v2u){0x3c00u, 0x3c00u})); PUT(8, q1); PUT(9, q2); PUT(1, p1); PUT(2, p2_); PUT(4, p4);
            SCHED();
            PUT(10, cmul(q2, q1)); PUT(5, cmul(p4, p1)); PUT(3, p3_);
            SCHED();
            PUT(6, cmul(p4, p2_)); PUT(7, cmul(p4, p3_));
            SCHED();
            STAMP(3);
            PAD(14);
            f32x16 kd = {0};
            {
                v4u pv[16], qv[16], ff[16];
#pragma unroll
                for (int s = 0; s < 4; s++) { pv[s] = lld<v4u>(pa + s * 32); qv[s] = lld<v4u>(qa + s * 32); }
#define KSTEP(s) { \
                    if ((s) + 4 < 16) { pv[((s) + 4) & 15] = lld<v4u>(pa + ((s) + 4) * 32); qv[((s) + 4) & 15] = lld<v4u>(qa + ((s) + 4) * 32); } \
                    ff[s] = cmulh4(pv[s], qv[s]); \
                    SCHED(); \
                    if ((s) > 0) kd = __builtin_amdgcn_mfma_f32_32x32x16_f16(__builtin_bit_cast(h8_t, ef[((s) - 1) & 15]), __builtin_bit_cast(h8_t, ff[((s) - 1) & 15]), kd, 0, 0, 0); \
                    if ((s) == 5 || (s) == 11) PAD(15 + (s) / 6); \
                    SCHED(); \
                }
                KSTEP(0) KSTEP(1) KSTEP(2) KSTEP(3) KSTEP(4) KSTEP(5) KSTEP(6) KSTEP(7) KSTEP(8) KSTEP(9) KSTEP(10) KSTEP(11) KSTEP(12) KSTEP(13) KSTEP(14) KSTEP(15)
                asm volatile("s_nop 1" : "+v"(ff[15]));
                kd = __builtin_amdgcn_mfma_f32_32x32x16_f16(__builtin_bit_cast(h8_t, ef[15]), __builtin_bit_cast(h8_t, ff[15]), kd, 0, 0, 0);
            }
            STAMP(4);
#pragma unroll
            for (int r = 0; r < 4; r++) kp[r] = pk(kd[2 * r + 1] * kscale, kd[2 * r] * kscale);
            kp[4] = pk(kd[8] * kscale, kd[9] * kscale); kp[5] = pk(kd[10] * kscale, kd[11] * kscale);
        }
        if (!role) {
            WAITF(F_TAG, 1);
            SETF(F_KG + wave, 1);
            tag = __builtin_amdgcn_readfirstlane(lld<u32>(lds + FL_OFF + F_TAGV * 4));
#pragma unroll
            for (int r = 0; r < 3; r++) __builtin_amdgcn_raw_buffer_store_b128((v4u){tag, kp[2 * r], tag, kp[2 * r + 1]}, rs, OFF_KT + ((ch >> 3) * 4 + (ch & 3)) * KTP + r * 1024 + lane * 16, 0, SC1);
            STAMP(11);
        }
    }
    asm volatile("" : "+s"(role));

    if (!role) {
        PAD(58);
        const int lane = lane_id(), i = lane & 15, g = lane >> 4;
        if (__builtin_expect(!has_w, 0)) {
            if (jx & 1) {
                if (wave < 4) END();
                PAD(69);
                WAITF(F_PUB, 1);
#pragma unroll 1
                for (int j = wave & 3; j < NPULL && wave < 7; j += 3) {
                    const int o = xcd * 14 + j, ct2 = SAMEX ? o >> 2 : (xcd >> 2) * 14 + j, kq2 = SAMEX ? o & 3 : kq;
                    dma4s(wd, (u32)((128 * ct2 + lane) * 2048 + 512 * kq2), lds0 + DUMP_OFF, false);
                    WAITV(0);
                }
                STAMP(21);
                STAMP_OUT();
                END();
            }
            const bool gates = xcd & 2;
            if (gates ? wave < 4 : (xcd & 1) != 0) END();
            PAD(61);
            __builtin_amdgcn_s_setprio(3);
            const int rho = (xcd & 4) * 2 + (wave ^ ((xcd & 1) * 4)), r2 = 2 * (lane & 31) + (lane >> 5);
            u32 lo = (u32)(2047 - 128 * rho - 2 * r2);
            int po = OFF_P + (gates * 16 + rho) * 4096 + lane * 16, oo = gates ? OFF_G + rho * 512 + r2 * 8 : OFF_F + (lane >> 3) * 256 + (xcd >> 2) * 128 + (lane & 7) * 16;
            asm volatile("" : "+v"(lo), "+v"(po), "+v"(oo));
            if (PREW && (gates || wave <= FREP)) {
                if (gates) __builtin_amdgcn_raw_buffer_store_b64((v2u){~0u, ~0u}, rs, oo, 0, SC1);
                else __builtin_amdgcn_raw_buffer_store_b128((v4u){~0u, ~0u, ~0u, ~0u}, rs, oo + wave * 2048, 0, SC1);
            }
            WAITF(F_PUB, 1);
            tag = __builtin_amdgcn_readfirstlane(lld<u32>(lds + FL_OFF + F_TAGV * 4));
            if (PTIME) __builtin_amdgcn_s_sleep(PTIME);
            STAMP(16);
            u32 p01 = dots<64>(rs, po, tag);
            STAMP(17);
            if (gates) {
                __builtin_amdgcn_raw_buffer_store_b64((v2u){tag, pk(sigm(bflo(p01)), sigm(bfhi(p01)))}, rs, oo, 0, SC1);
                STAMP(19);
                STAMP_OUT();
                END();
            }
            p01 ^= 0x80008000u | (((p01 >> 15) & 0x10001u) * 0x7fffu);
            u32 ka = (p01 << 16) | lo, kb = (p01 & 0xffff0000u) | (lo - 1u);
            PADV(65, ka);
            top8<true>(ka, kb, lane);
            if (lane < 4) lst<v2u>(lds + KEY_OFF + wave * 32 + lane * 8, (v2u){ka, kb});
            CFENCE();
            STAMP(18);
            PAD(66);
            const u32 nth = __builtin_amdgcn_readfirstlane(lane == 0 ? __hip_atomic_fetch_add((LAS u32*)(lds + FL_OFF + F_XS * 4), 1u, __ATOMIC_RELAXED, __HIP_MEMORY_SCOPE_WORKGROUP) : 0u);
            const v2u kk = lld<v2u>(lds + KEY_OFF + (lane & 31) * 8);
            if (nth == 7) {
                ka = kk.x; kb = kk.y;
                top8<false>(ka, kb, lane);
#pragma unroll
                for (int r = 0; r <= FREP; r++) __builtin_amdgcn_raw_buffer_store_b128((v4u){tag, ka, tag, kb}, rs, oo + r * 2048, 0, SC1);
                STAMP(20);
            }
            STAMP_OUT();
            END();
        }
        if (wave >= 4) {
            if (wave != 7) END();
            PAD(62);
            const int fo = OFF_F + (jx % FREP) * 2048 + xcd * 256;
            u32 xs = (u32)(2047 * 2048 + 512 * kq + (((lane & 7) ^ ((lane >> 3) & 6)) << 4)), key, miss;
            asm volatile("" : "+v"(xs));
            if (RTIME) { while ((u32)__builtin_amdgcn_s_memrealtime() - tstart < (u32)RTIME) __builtin_amdgcn_s_sleep(1); }
            STAMP(18);
            asm volatile("s_movk_i32 s80, 64\n"
                         "GMX_FP:\n\ts_load_dwordx16 s[48:63], %2, 0 glc\n\ts_load_dwordx16 s[64:79], %2, 128 glc\n\ts_waitcnt lgkmcnt(0)\n\t"
                         "s_xor_b32 s48, s48, %3\n\t""s_xor_b32 s50, s50, %3\n\t""s_xor_b32 s52, s52, %3\n\t""s_xor_b32 s54, s54, %3\n\t""s_xor_b32 s56, s56, %3\n\t""s_xor_b32 s58, s58, %3\n\t""s_xor_b32 s60, s60, %3\n\t""s_xor_b32 s62, s62, %3\n\t"
                         "s_xor_b32 s64, s64, %3\n\t""s_xor_b32 s66, s66, %3\n\t""s_xor_b32 s68, s68, %3\n\t""s_xor_b32 s70, s70, %3\n\t""s_xor_b32 s72, s72, %3\n\t""s_xor_b32 s74, s74, %3\n\t""s_xor_b32 s76, s76, %3\n\t""s_xor_b32 s78, s78, %3\n\t"
                         "s_or_b32 s48, s48, s50\n\t""s_or_b32 s52, s52, s54\n\t""s_or_b32 s56, s56, s58\n\t""s_or_b32 s60, s60, s62\n\t""s_or_b32 s64, s64, s66\n\t""s_or_b32 s68, s68, s70\n\t""s_or_b32 s72, s72, s74\n\t""s_or_b32 s76, s76, s78\n\t"
                         "s_or_b32 s48, s48, s52\n\t""s_or_b32 s56, s56, s60\n\t""s_or_b32 s64, s64, s68\n\t""s_or_b32 s72, s72, s76\n\t"
                         "s_or_b32 s48, s48, s56\n\t""s_or_b32 s64, s64, s72\n\t"
                         "s_or_b32 %1, s48, s64\n\ts_cbranch_scc0 GMX_FD\n\t"
                         "s_sleep 1\n\ts_addk_i32 s80, -1\n\ts_cmp_lg_u32 s80, 0\n\ts_cbranch_scc1 GMX_FP\n"
                         "GMX_FD:\n\t""s_max_u32 s49, s49, s79\n\t""s_max_u32 s51, s51, s77\n\t""s_max_u32 s53, s53, s75\n\t""s_max_u32 s55, s55, s73\n\t""s_max_u32 s57, s57, s71\n\t""s_max_u32 s59, s59, s69\n\t""s_max_u32 s61, s61, s67\n\t""s_max_u32 s63, s63, s65\n\t"
                         "v_mov_b32 %0, s49\n\ts_lshl_b64 exec, exec, 8\n\t""v_mov_b32 %0, s51\n\ts_lshl_b64 exec, exec, 8\n\t""v_mov_b32 %0, s53\n\ts_lshl_b64 exec, exec, 8\n\t""v_mov_b32 %0, s55\n\ts_lshl_b64 exec, exec, 8\n\t"
                         "v_mov_b32 %0, s57\n\ts_lshl_b64 exec, exec, 8\n\t""v_mov_b32 %0, s59\n\ts_lshl_b64 exec, exec, 8\n\t""v_mov_b32 %0, s61\n\ts_lshl_b64 exec, exec, 8\n\t""v_mov_b32 %0, s63\n\ts_mov_b64 exec, -1"
                         : "=&v"(key), "=&s"(miss) : "s"(ws + fo), "s"(tag)
                         : "memory", "scc", "s48", "s49", "s50", "s51", "s52", "s53", "s54", "s55", "s56", "s57", "s58", "s59", "s60", "s61", "s62", "s63", "s64", "s65", "s66", "s67", "s68", "s69", "s70", "s71", "s72", "s73", "s74", "s75", "s76", "s77", "s78", "s79", "s80");
            if (__builtin_expect(miss != 0u, 0)) {
                COLD_BEGIN();
#pragma unroll 1
                for (int spins = 0; spins < SPIN_CAP; ++spins) {
                    CFENCE();
                    const v2u q = __builtin_amdgcn_raw_buffer_load_b64(rs, fo + (lane >> 3) * 8, 0, SC1), r = __builtin_amdgcn_raw_buffer_load_b64(rs, fo + 184 - (lane >> 3) * 8, 0, SC1);
                    key = umax(q.y, r.y);
                    if (__all((q.x == tag) & (r.x == tag))) break;
                }
                COLD_END();
            }
            STAMP(19);
            xs -= (key & 2047u) * 2048u;
#pragma unroll
            for (int p = 0; p < 4; p++) dma16cs(x + 128 * p, xs, lds0 + XS_OFF + p * 1024);
            SCHED();
            lst<u32>(lds + KEY_OFF + (lane >> 3) * 4, key);
            if (pb == 0 && lane == 0) __builtin_amdgcn_raw_buffer_store_b32(EPOCH() + 1u, rs, OFF_E, 0, SC1);
            WAITV(0);
            SETF(F_XS, 1);
            STAMP(5);
            STAMP_OUT();
            END();
        }
        PAD(40);
        __builtin_amdgcn_s_setprio(3);
        const int un = wave & 3, hi = i >> 3, row = 4 * (g & 1) + (i >> 2);
        int po = OFF_H2 + ((un & 2) ? ctu : ct) * 16384 + (((un & 1) * 2 + (lane >> 5)) * 4 + kq) * 1024 + (un >> 1) * 512 + (lane & 31) * 16;
        int ho = OFF_H3 + (32 * kq + 8 * un + 4 * hi + (g >> 1)) * 3584 + ct * 128 + (i & 7) * 16 + (g & 1) * 8;
        lptr gp[4], fl = lds + FL_OFF + (F_GU + (un >> 1)) * 4;
#pragma unroll
        for (int e = 0; e < 4; e++) gp[e] = lds + GU_OFF + un * 1024 + (g >> 1) * 4096 + row * 128 + ((e ^ (row >> 1)) << 5) + (i & 3) * 8;
        f32x4 acc[4];
        v4u wf[8][4], xb[8];
#define RDW(s) _Pragma("unroll") for (int e = 0; e < 4; e++) { const v2u lo = ldtr(gp[e] + (s) * 16384), hh = ldtr(gp[e] + (s) * 16384 + 8192); wf[s][e] = (v4u){lo.x, lo.y, hh.x, hh.y}; }
#define MMW(s) _Pragma("unroll") for (int e = 0; e < 4; e++) acc[e] = mfma16(wf[s][e], xb[s], (s) ? acc[e] : (f32x4){0, 0, 0, 0});
        asm volatile("" : "+v"(po), "+v"(ho), "+v"(fl));
        waitf(fl, 5);
        RDW(0) RDW(1) RDW(2) RDW(3)
        SETF(F_FR + un, 1);
        PAD(42);
        waitf(fl, 8);
        RDW(4) RDW(5) RDW(6)
        STAMP(14);
        bool late = true;
#pragma unroll 1
        for (int spins = 0; spins < LSPIN_CAP; ++spins) {
            CFENCE();
            const u32 a = __builtin_amdgcn_readfirstlane(lld<u32>(fl)), b = __builtin_amdgcn_readfirstlane(lld<u32>(lds + FL_OFF + F_XS * 4));
            late = a < 9;
            if (!late || b) break;
            __builtin_amdgcn_s_sleep(1);
        }
        CFENCE();
        if (!late) { RDW(7) WAITF(F_XS, 1); }
        STAMP(15);
        PAD(43);
#pragma unroll
        for (int s = 0; s < 8; s++) xb[s] = lld<v4u>(lds + XS_OFF + (s >> 1) * 1024 + (i & 7) * 128 + (((4 * (s & 1) + g) ^ (i & 6)) << 4));
        const u32 sk = lld<u32>(lds + KEY_OFF + (i & 7) * 4) >> 16;
        SCHED();
#pragma unroll
        for (int s = 0; s < 7; s++) MMW(s)
        SCHED();
        if (late) { waitf(fl, 9); RDW(7) }
        MMW(7)
        PAD(44);
        {
            f32x4 a0, a1;
#pragma unroll
            for (int r = 0; r < 4; r++) { a0[r] = hi ? acc[2][r] : acc[0][r]; a1[r] = hi ? acc[3][r] : acc[1][r]; }
            __builtin_amdgcn_raw_buffer_store_b128((v4u){pkd(a0[0], a0[1]), pkd(a0[2], a0[3]), pkd(a1[0], a1[1]), pkd(a1[2], a1[3])}, rs, po, 0, SC1);
        }
        STAMP(7);
        float rsel = rbf(sigm(__uint_as_float(((sk & 0x8000u) ? (sk ^ 0x8000u) : (sk ^ 0xffffu)) << 16)));
        asm volatile("" : "+v"(rsel));
        CFENCE();
        late = __builtin_amdgcn_readfirstlane(lld<u32>(fl)) < 13;
        CFENCE();
        if (!late) { RDW(0) RDW(1) RDW(2) RDW(3) }
        if (H2DELAY) __builtin_amdgcn_s_sleep(H2DELAY);
        {
            v4u q[4];
            fetch<4, 1024, false, 83>(q, rs, OFF_H2 + ct * 16384 + un * 4096 + lane * 16, 0);
            STAMP(6);
            float hv[4];
            f32x2 sm[4];
#pragma unroll
            for (int d = 0; d < 4; d++) sm[d] = ((f32x2){bflo(q[0][d]), bfhi(q[0][d])} + (f32x2){bflo(q[1][d]), bfhi(q[1][d])}) + ((f32x2){bflo(q[2][d]), bfhi(q[2][d])} + (f32x2){bflo(q[3][d]), bfhi(q[3][d])});
            PADV(85, sm[0]);
#pragma unroll
            for (int r = 0; r < 4; r++) {
                const auto sw = __builtin_amdgcn_permlane32_swap(__float_as_uint(sm[r >> 1][r & 1]), __float_as_uint(sm[2 + (r >> 1)][r & 1]), false, false);
                const float gr = rbf(__uint_as_float(sw[0]));
                hv[r] = rbf(gr * sigm(gr)) * rbf(__uint_as_float(sw[1]));
            }
            lst<v2u>(lds + HS_OFF + (i & 7) * 272 + (un >> 1) * 128 + hi * 64 + (lane >> 5) * 32 + (un & 1) * 16 + (g & 1) * 8, (v2u){pk(hv[0], hv[1]), pk(hv[2], hv[3])});
            SETF(F_H + un, 1);
        }
        PAD(86);
        if (late) { waitf(fl, 13); RDW(0) RDW(1) RDW(2) RDW(3) }
        PAD(87);
        waitf(lds + FL_OFF + (F_H + (lane & 3)) * 4, 1);
        STAMP(8);
#pragma unroll
        for (int s = 0; s < 4; s++) xb[s] = lld<v4u>(lds + HS_OFF + (i & 7) * 272 + 64 * s + 16 * g);
        MMW(0) MMW(1) MMW(2) MMW(3)
#pragma unroll
        for (int e = 0; e < 2; e++) {
            f32x4 v;
#pragma unroll
            for (int r = 0; r < 4; r++) v[r] = (hi ? acc[2 + e][r] : acc[e][r]) * rsel;
            __builtin_amdgcn_raw_buffer_store_b64((v2u){pkd(v[0], v[1]), pkd(v[2], v[3])}, rs, ho + e * 7168, 0, SC1);
        }
        STAMP(9);
        STAMP_OUT();
        END();
    }

    PAD(0);
    const bool isb = wave >= 4;
    if (isb) {
        PAD(30);
        const int wq = wave & 3;
        dsk = *(const __attribute__((address_space(4))) u32*)(u64)(dskip + (ch >> 1) * 4);
        u32 xo = (u32)((wq * 128 + lane) * 8192 + pb * 16);
        asm volatile("" : "+v"(xo));
        if (wq < 2) __builtin_amdgcn_s_setprio(3); else __builtin_amdgcn_s_setprio(1);
#pragma unroll 1
        for (int m = wq; m < 5 && wq < 2; m += 2) lst<v4u>(lds + MSK_OFF + m * 1024 + lane * 16, group_mask(m, lane));
        WAITF(F_PUB, 1);
        tag = __builtin_amdgcn_readfirstlane(lld<u32>(lds + FL_OFF + F_TAGV * 4));
        if (wq >= 2) __builtin_amdgcn_s_sleep(XDELAY);
        STAMP(1);
        v4u xc[8], gq[4], kq3[3];
#pragma unroll
        for (int k = 0; k < 8; k++) { xc[k] = __builtin_amdgcn_raw_buffer_load_b128(rx, xo + (k & 1) * 2048, (k >> 2) * 524288 + (k & 2) * 2048, NT); SCHED(); }
        STAMP(2);
        PAD(31);
#pragma unroll
        for (int t = 0; t < 2; t++)
#pragma unroll
            for (int e = 0; e < 8; e++) {
                const u32 sel = (e & 1) ? 0x07060302u : 0x05040100u;
                lst<v2u>(lds + e * UP + U_OFF + t * 512 + (wq * 128 + lane) * 8, (v2u){__builtin_amdgcn_perm(xc[4 * t + 1][e >> 1], xc[4 * t][e >> 1], sel), __builtin_amdgcn_perm(xc[4 * t + 3][e >> 1], xc[4 * t + 2][e >> 1], sel)});
            }
        SETF(F_UG + wq + (lane & 4), 2);
        STAMP(5);
        __builtin_amdgcn_s_setprio(1);
        CONV_SETUP();
        SCHED();
        PAD(32);
        if (wq < 2) {
            while ((u32)__builtin_amdgcn_s_memrealtime() - tstart < (u32)GTIME) __builtin_amdgcn_s_sleep(1);
            STAMP(3);
            fetch<4, 1024, true, 24>(gq, rs, OFF_G + wq * 4096 + lane * 16, tag);
#pragma unroll
            for (int t = 0; t < 4; t++) lst<f32x4>(lds + G_OFF + wq * 4096 + t * 1024 + lane * 16, (f32x4){bflo(gq[t].y), bfhi(gq[t].y), bflo(gq[t].w), bfhi(gq[t].w)});
            SETF(F_GT + wq + (lane & 6), 1);
            STAMP(16);
        }
        fetch<3, 1024, true, 26>(kq3, rs, OFF_KT + (pb * 4 + wq) * KTP + lane * 16, tag);
        if (CPULL == 1 && has_w && wave != 7) {
            dma4s(wd, (u32)((128 * ct + 64 + lane) * 2048 + 512 * kq + 128 * wq), lds0 + DUMP_OFF, false);
            if (wq == 0) dma4s(wd, (u32)((128 * ct + 64 + lane) * 2048 + 512 * kq + 384), lds0 + DUMP_OFF, false);
        }
#pragma unroll
        for (int r = 0; r < 3; r++) { kp[2 * r] = kq3[r].y; kp[2 * r + 1] = kq3[r].w; }
        STAMP(4);
        if (wave == 7) {
            const int fo = OFF_F2 + xcd * 256 + (lane & 7) * 8;
            v2u fq, fr;
            while ((u32)__builtin_amdgcn_s_memrealtime() - tstart < (u32)FTIME) __builtin_amdgcn_s_sleep(2);
#pragma unroll 1
            for (int spins = 0; spins < SPIN_CAP; ++spins) {
                PAD(27);
                CFENCE();
                fq = __builtin_amdgcn_raw_buffer_load_b64(rs, fo, 0, SC1); fr = __builtin_amdgcn_raw_buffer_load_b64(rs, fo ^ 184, 0, SC1);
                if (__all((fq.x == tag) & (fr.x == tag))) break;
                __builtin_amdgcn_s_sleep(2);
            }
            const u32 rrow = 2047u - (umax(fq.y, fr.y) & 2047u);
            if (lane < 8) {
                __hip_atomic_fetch_or((LAS u32*)(lds + SKIP_OFF + (rrow >> 5) * 4), 1u << (rrow & 31), __ATOMIC_RELAXED, __HIP_MEMORY_SCOPE_WORKGROUP);
                lst<u32>(lds + SKIP_OFF + 256 + lane * 4, rrow);
            }
            SETF(F_SKIP, 1);
            STAMP(17);
        }
    }
    PAD(17);
    {
        lst<v4u>(tn + 1024, (v4u){__builtin_amdgcn_alignbit(kp[1], kp[2], 16), kp[1], __builtin_amdgcn_alignbit(kp[0], kp[1], 16), kp[0]});
        if (lane == 0) lst<u32>(tl + 548 * 4, kp[0] >> 16);
        const u32 k77 = __builtin_amdgcn_perm(kp[3], kp[3], 0x01000100u);
        lst<v4u>(tn, (v4u){k77, kp[3], __builtin_amdgcn_alignbit(kp[2], kp[3], 16), __builtin_amdgcn_perm(kp[2], kp[2], 0x01000100u)});
        if (lane == 63) lst<v4u>(tl + 32 * 4, (v4u){0u, k77 & 0xffff0000u, k77, k77});
        const u32 s2 = kp[4], s3 = kp[5];
        lst<u16>(t2 + 2, (u16)s2); lst<u16>(t2 + 322, (u16)(s2 >> 16)); lst<u16>(t3 + 66, (u16)s3); lst<u16>(t3 + 2, (u16)(s3 >> 16));
        CFENCE();
        lst<u16>(t2, (u16)s2); lst<u16>(t2 + 320, (u16)(s2 >> 16)); lst<u16>(t3 + 64, (u16)s3); lst<u16>(t3, (u16)(s3 >> 16));
    }
    const float dm1 = __uint_as_float(__builtin_amdgcn_readfirstlane(__float_as_uint(((wave & 1) ? bfhi(dsk) : bflo(dsk)) - 1.f)));
    STAMP(6);
    waitf((lptr)((u32)(uintptr_t)pr & ~24u) + F_UG * 4, 2);
    PAD(23);
    STAMP(8); STAMPC(22);
    asm volatile("s_waitcnt lgkmcnt(0)\n" CONV_LOOP :: [ub] "v"(ub), [eb] "v"(eb), [sb] "v"(sb), [gb] "v"(gb), [pg] "v"(pg), [dm] "s"(dm1), [pn] "v"(pn), [p2] "v"(p2),
                 [ma] "v"(ma), [pr] "v"(pr), [k1] "s"(tl + T_P2 + (3 * 80 + 84 - 63) * 4) LOOP_BRKS : LOOP_CLOB);
    STAMP(9); STAMPC(23);
    PAD(140);
    if (isb) { STAMP(11); STAMP_OUT(); END(); }
    const int ln = lane_id();
    if (wave != 0) {
        const lptr ob = lds + S_OFF + ((ln >> 2) & 3) * UP + 32 * (ln >> 4), pf = lds + PR_OFF + (ln & 7) * 4;
        WAITF(F_SKIP, 1);
#pragma unroll 1
        for (int c = wave - 1; c < 32; c += 3) {
            const u32 sk = lld<u32>(lds + SKIP_OFF + c * 8 + (ln >> 5) * 4);
            waitf(pf + (c >> 2) * 32, 1);
            const lptr o = ob + 128 * c + 8 * ((ln & 3) ^ (c & 3));
            const v2u a0 = ldtr(o), a1 = ldtr(o + 4 * UP);
            if (!((sk >> (ln & 31)) & 1u)) *(GAS v4u*)(out + (u32)((64 * c + ln) * 2048 + 16 * pb)) = (v4u){a0.x, a0.y, a1.x, a1.y};
        }
        STAMP(11);
        STAMP_OUT();
        END();
    }
    PAD(141);
    WAITF(F_SKIP, 1);
    const u32 rrow = lld<u32>(lds + SKIP_OFF + 256 + (ln & 7) * 4);
    {
        const int ho = OFF_H3 + pb * 3584 + ln * 16, h3 = ln < 32 ? ho + 3072 : WS_USED;
        v4u q[4];
        const float omg = 1.f - lld<float>(lds + G_OFF + rrow * 4);
        while ((u32)__builtin_amdgcn_s_memrealtime() - tstart < (u32)HTIME) __builtin_amdgcn_s_sleep(2);
        bool ok = false;
#pragma unroll 1
        for (int spins = 0; spins < SPIN_CAP; ++spins) {
            PAD(142);
            CFENCE();
            if (!ok) {
#pragma unroll
                for (int k = 0; k < 3; k++) q[k] = __builtin_amdgcn_raw_buffer_load_b128(rs, ho, k * 1024, SC1);
                q[3] = __builtin_amdgcn_raw_buffer_load_b128(rs, h3, 0, SC1);
            }
            ok = arrived<4, false>(q, 0);
            if (__all(ok)) break;
            if (PSLEEP) __builtin_amdgcn_s_sleep(PSLEEP);
        }
        STAMP(10);
        PAD(143);
        f32x2 y[4];
#pragma unroll
        for (int d = 0; d < 4; d++) y[d] = ((f32x2){bflo(q[0][d]), bfhi(q[0][d])} + (f32x2){bflo(q[1][d]), bfhi(q[1][d])}) + ((f32x2){bflo(q[2][d]), bfhi(q[2][d])} + (f32x2){bflo(q[3][d]), bfhi(q[3][d])});
        float z[4], z2[2];
#pragma unroll
        for (int d = 0; d < 4; d++) {
            const auto sw = __builtin_amdgcn_permlane32_swap(__float_as_uint(y[d >> 1][d & 1]), __float_as_uint(y[2 + (d >> 1)][d & 1]), false, false);
            z[d] = __uint_as_float(sw[0]) + __uint_as_float(sw[1]);
        }
#pragma unroll
        for (int d = 0; d < 2; d++) {
            const auto sw = __builtin_amdgcn_permlane16_swap(__float_as_uint(z[d]), __float_as_uint(z[2 + d]), false, false);
            z2[d] = __uint_as_float(sw[0]) + __uint_as_float(sw[1]);
        }
        const float ys = ((ln & 8) ? z2[1] : z2[0]) + __uint_as_float(DPP(__float_as_uint((ln & 8) ? z2[0] : z2[1]), 0x128));
        const int e = ln >> 3;
        waitf(lds + PR_OFF + (rrow >> 8) * 32 + e * 4, 1);
        const float base = __uint_as_float((u32)lld<u16>(lds + S_OFF + e * UP + ((rrow * 2) ^ ((rrow >> 3) & 24))) << 16);
        const float o = base + omg * ys;
        *(GAS u16*)(out + (rrow * 2048 + 16 * pb + 2 * e)) = (u16)pk(o, o);
    }
    STAMP(11);
    STAMP_OUT();
    END();
}

extern "C" void kernel_launch(void* const* d_in, const int* in_sizes, int n_in, void* d_out, int out_size, void* d_ws,
                              size_t ws_size, hipStream_t stream) {
    fused_kernel<<<256, 512, 0, stream>>>(d_in[0], d_in[8], d_in[12], (char*)d_ws + WS_BASE, d_in[9], d_in[10], d_in[5], d_in[6],
                                          d_in[1], d_in[2], d_in[3], d_in[4], d_in[11], d_out, d_in[0], d_in[0]);
}
```
